```python
import torch

def _softmax(x, axis=-1):
    return torch.softmax(x, dim=axis)
def _gelu(x):
    return torch.nn.functional.gelu(x, approximate="tanh")
def _silu(x):
    return torch.nn.functional.silu(x)
def _sigmoid(x):
    return torch.sigmoid(x)
def _layernorm(x, axis=-1, eps=1e-5):
    m = x.mean(dim=axis, keepdim=True)
    v = x.var(dim=axis, keepdim=True, unbiased=False)
    return (x - m) / torch.sqrt(v + eps)
def _scatter_add(tgt, idx, src, axis=0):
    return tgt.clone().index_add_(axis, idx, src)
def _segment_sum(src, seg, n):
    out = torch.zeros((n,) + src.shape[1:], dtype=src.dtype)
    return out.index_add_(0, seg, src)
def _pad(x, pw):
    flat = tuple(v for pair in reversed(pw) for v in pair)
    return torch.nn.functional.pad(x, flat)

kernel_name = 'standalone__deep_cross_xdeepfm__recsys'

def setup_inputs():

    _w = (lambda a: (lambda t: t.to(torch.bfloat16) if t.is_floating_point() else t)(torch.as_tensor(a)))
    B, F, D, L = 16384, 26, 16, 3
    V = min(1000, 100000)
    Din = F * D
    return {
        "idx": _w((lambda lo, hi, *s: torch.randint(lo, hi, s))(0, V, B, F).to(torch.int64)),
        "table": _w((lambda *s: torch.randn(*s, dtype=torch.bfloat16) * 0.8)(V, D)),
        "ws_cin": _w((lambda *s: torch.randn(*s, dtype=torch.bfloat16) * 0.2)(L, F, F, F) * (1.0 / F)),
        "w_cin_out": _w((lambda *s: torch.randn(*s, dtype=torch.bfloat16) * 0.2)(L * F) * ((L * F) ** -0.5)),
        "w_dnn1": _w((lambda *s: torch.randn(*s, dtype=torch.bfloat16) * 0.8)(Din, Din) * (Din ** -0.5)),
        "b_dnn1": _w((lambda *s: torch.randn(*s, dtype=torch.bfloat16) * 3.2)(Din) * 0.1),
        "w_dnn2": _w((lambda *s: torch.randn(*s, dtype=torch.bfloat16) * 0.2)(Din) * (Din ** -0.5)),
        "b_out": _w((lambda *s: torch.randn(*s, dtype=torch.bfloat16) * 0.2)(1)),
    }

def reference(idx, table, ws_cin, w_cin_out, w_dnn1, b_dnn1, w_dnn2, b_out):
    x0 = table[idx]
    B = x0.shape[0]
    L = ws_cin.shape[0]
    xk = x0
    pooled = []
    for l in range(L):
        xk = torch.einsum('bid,bjd,hij->bhd', xk, x0, ws_cin[l])
        pooled.append(xk.sum(axis=-1))
    p_plus = torch.stack(pooled, axis=1).reshape(B, -1)
    logit_cin = p_plus @ w_cin_out
    h = x0.reshape(B, -1)
    h = _gelu(h @ w_dnn1 + b_dnn1)
    logit_dnn = h @ w_dnn2
    return logit_cin + logit_dnn + b_out
```

```cpp
#include <hip/hip_runtime.h>
#include <hip/hip_bf16.h>
__device__ __forceinline__ float bf2f(unsigned short u){ return __uint_as_float(((unsigned)u)<<16); }
__global__ __launch_bounds__(256) void naive_kernel(const int* idx, const unsigned short* table, const unsigned short* wc,
    const unsigned short* wco, const unsigned short* w1, const unsigned short* b1, const unsigned short* w2, const unsigned short* bo,
    __hip_bfloat16* out) {
  __shared__ float x0[16][26][16];
  __shared__ float xa[16][26][16];
  __shared__ float xb[16][26][16];
  __shared__ float red[16][16];
  int t = threadIdx.x; int s = t >> 4, d = t & 15; int b = blockIdx.x * 16 + s;
  for (int f = 0; f < 26; ++f) { float v = bf2f(table[idx[b*26+f]*16 + d]); x0[s][f][d] = v; xa[s][f][d] = v; }
  __syncthreads();
  float acc = 0.f;
  for (int l = 0; l < 3; ++l) {
    float (*src)[26][16] = (l & 1) ? xb : xa;
    float (*dst)[26][16] = (l & 1) ? xa : xb;
    for (int h = 0; h < 26; ++h) {
      float v = 0.f;
      for (int i = 0; i < 26; ++i) { float xi = src[s][i][d]; float u = 0.f;
        for (int j = 0; j < 26; ++j) u += x0[s][j][d] * bf2f(wc[((l*26+h)*26+i)*26+j]);
        v += xi * u; }
      dst[s][h][d] = v;
      acc += v * bf2f(wco[l*26+h]);
    }
  }
  for (int n = d; n < 416; n += 16) {
    float h = bf2f(b1[n]);
    for (int k = 0; k < 416; ++k) h += x0[s][k>>4][k&15] * bf2f(w1[k*416+n]);
    float u = 0.7978845608028654f * (h + 0.044715f*h*h*h);
    float g = 0.5f*h*(1.f+tanhf(u));
    acc += g * bf2f(w2[n]);
  }
  red[s][d] = acc;
  __syncthreads();
  if (d == 0) { float r = bf2f(bo[0]); for (int k = 0; k < 16; ++k) r += red[s][k]; out[b] = __float2bfloat16(r); }
}
extern "C" void kernel_launch(void* const* d_in, const int* in_sizes, int n_in, void* d_out, int out_size, void* d_ws, size_t ws_size, hipStream_t stream) {
  naive_kernel<<<16384/16, 256, 0, stream>>>((const int*)d_in[0], (const unsigned short*)d_in[1], (const unsigned short*)d_in[2],
     (const unsigned short*)d_in[3], (const unsigned short*)d_in[4], (const unsigned short*)d_in[5], (const unsigned short*)d_in[6], (const unsigned short*)d_in[7], (__hip_bfloat16*)d_out);
}
```

```cpp
#include <hip/hip_runtime.h>
#include <hip/hip_bf16.h>
#include <stdint.h>

typedef unsigned u32x4 __attribute__((ext_vector_type(4)));
typedef float f32x4 __attribute__((ext_vector_type(4)));
typedef float f32x16 __attribute__((ext_vector_type(16)));
typedef __bf16 bf16x8 __attribute__((ext_vector_type(8)));
typedef _Float16 f16x2 __attribute__((ext_vector_type(2)));
typedef unsigned short u16;
typedef u16 u16x2 __attribute__((ext_vector_type(2)));
typedef unsigned long long u64;
typedef __attribute__((address_space(1))) u64 gu64;
#define LDSP(T, off) ((__attribute__((address_space(3))) T*)(uintptr_t)(unsigned)(off))
#define DI __device__ __forceinline__
#define RLX __ATOMIC_RELAXED, __HIP_MEMORY_SCOPE_AGENT

constexpr int ROWSTR = 784, CONSTROW = 32000, LAND = 32032, ZROW = 52416, OFFS = 53184, IDX16 = 93504, DPART = 104256,
              FLAGS = 61376, TB = 61504, CELLS = 100160, LDS_TOTAL = 163648;
constexpr int WS_FRAG = 0, WS_EPOCH = 40960, WS_M3 = 45056, WS_ACK = 65536;

__device__ const u16 SLOT_TAB[48 * 16] = {
  256,8, 384,12, 260,136, 388,140, 768,24, 896,28, 772,152, 900,156,
  516,144, 644,148, 512,16, 640,20, 1540,176, 1668,180, 1536,48, 1664,52,
  1024,32, 1152,36, 1028,160, 1156,164, 2304,72, 2432,76, 2308,200, 2436,204,
  2052,192, 2180,196, 2048,64, 2176,68, 1284,168, 1412,172, 1280,40, 1408,44,
  2560,80, 2688,84, 396,108, 924,108, 2564,208, 2692,212, 3072,96, 3200,100,
  1796,184, 1924,188, 660,108, 1716,108, 1792,56, 1920,60, 2820,216, 2948,220,
  3076,224, 3204,228, 288,1032, 416,1036, 292,1160, 420,1164, 336,2568, 464,2572,
  2816,88, 2944,92, 576,2064, 704,2068, 580,2192, 708,2196, 568,1808, 696,1812,
  340,2696, 468,2700, 1188,108, 2508,108, 800,1048, 928,1052, 804,1176, 932,1180,
  572,1936, 700,1940, 2244,108, 1452,108, 1600,2096, 1728,2100, 1604,2224, 1732,2228,
  864,3096, 992,3100, 868,3224, 996,3228, 2384,2632, 2512,2636, 2388,2760, 2516,2764,
  1624,2864, 1752,2868, 1628,2992, 1756,2996, 1336,1832, 1464,1836, 1340,1960, 1468,1964,
  2400,3144, 2528,3148, 2772,108, 3300,108, 2404,3272, 2532,3276, 0,108, 128,4,
  1368,2856, 1496,2860, 1980,108, 3036,108, 1372,2984, 1500,2988, 132,108, 108,108,
  264,108, 392,268, 792,108, 920,796, 1056,108, 1184,1060, 2376,108, 2504,2380,
  528,108, 656,532, 1584,108, 1712,1588, 2112,108, 2240,2116, 1320,108, 1448,1324,
  2640,108, 2768,2644, 3168,108, 3296,3172, 272,520, 400,524, 276,648, 404,652,
  1848,108, 1976,1852, 2904,108, 3032,2908, 544,1040, 672,1044, 548,1168, 676,1172,
  296,1288, 424,1292, 300,1416, 428,1420, 344,2824, 472,2828, 348,2952, 476,2956,
  592,2576, 720,2580, 596,2704, 724,2708, 584,2320, 712,2324, 588,2448, 716,2452,
  536,784, 664,788, 540,912, 668,916, 608,3088, 736,3092, 612,3216, 740,3220,
  1072,1568, 1200,1572, 1076,1696, 1204,1700, 1112,2848, 1240,2852, 1116,2976, 1244,2980,
  816,1560, 944,1564, 820,1688, 948,1692, 824,1816, 952,1820, 828,1944, 956,1948,
  1632,3120, 1760,3124, 1636,3248, 1764,3252, 1544,304, 1672,308, 1548,432, 1676,436,
  1064,1312, 1192,1316, 1068,1440, 1196,1444, 1080,1824, 1208,1828, 1084,1952, 1212,1956,
  2128,2624, 2256,2628, 2132,2752, 2260,2756, 2056,320, 2184,324, 2060,448, 2188,452,
  1088,2080, 1216,2084, 1092,2208, 1220,2212, 1352,2344, 1480,2348, 1356,2472, 1484,2476,
  2072,832, 2200,836, 2076,960, 2204,964, 2600,1360, 2728,1364, 2604,1488, 2732,1492,
  1608,2352, 1736,2356, 1612,2480, 1740,2484, 1616,2608, 1744,2612, 1620,2736, 1748,2740,
  3112,1376, 3240,1380, 3116,1504, 3244,1508, 3128,1888, 3256,1892, 3132,2016, 3260,2020,
  1872,2616, 2000,2620, 1876,2744, 2004,2748, 2120,2368, 2248,2372, 2124,2496, 2252,2500,
  312,1800, 440,1804, 316,1928, 444,1932, 808,1304, 936,1308, 812,1432, 940,1436,
  2144,3136, 2272,3140, 2148,3264, 2276,3268, 2648,2896, 2776,2900, 2652,3024, 2780,3028,
  856,2840, 984,2844, 860,2968, 988,2972, 1864,2360, 1992,2364, 1868,2488, 1996,2492,
  2912,3160, 3040,3164, 2916,3288, 3044,3292, 552,1296, 680,1300, 556,1424, 684,1428,
  2392,2888, 2520,2892, 2396,3016, 2524,3020, 1104,2592, 1232,2596, 1108,2720, 1236,2724,
  560,1552, 688,1556, 564,1680, 692,1684, 600,2832, 728,2836, 604,2960, 732,2964,
  1120,3104, 1248,3108, 1124,3232, 1252,3236, 1096,2336, 1224,2340, 1100,2464, 1228,2468,
  1328,1576, 1456,1580, 1332,1704, 1460,1708, 1344,2088, 1472,2092, 1348,2216, 1476,2220,
  2656,3152, 2784,3156, 2660,3280, 2788,3284, 2584,848, 2712,852, 2588,976, 2716,980,
  1592,1840, 1720,1844, 1596,1968, 1724,1972, 1856,2104, 1984,2108, 1860,2232, 1988,2236,
  3080,352, 3208,356, 3084,480, 3212,484, 280,776, 408,780, 284,904, 412,908,
  1880,2872, 2008,2876, 1884,3000, 2012,3004, 2136,2880, 2264,2884, 2140,3008, 2268,3012,
  328,2312, 456,2316, 332,2440, 460,2444, 840,2328, 968,2332, 844,2456, 972,2460,
  104,108, 232,108, 360,108, 488,108, 872,108, 1000,108, 1128,108, 1256,108,
  108,108, 108,108, 616,108, 744,108, 1640,108, 1768,108, 2152,108, 2280,108,
  2408,108, 2536,108, 2664,108, 2792,108, 3176,108, 3304,108, 112,108, 108,108,
  1384,108, 1512,108, 1896,108, 2024,108, 2920,108, 3048,108, 108,108, 108,108
};

DI float bflo(unsigned v) { return __uint_as_float(v << 16); }
DI float bfhi(unsigned v) { return __uint_as_float(v & 0xffff0000u); }
DI float bf1(u16 v) { return __uint_as_float((unsigned)v << 16); }
DI unsigned pkbf(float a, float b) { typedef __bf16 b2 __attribute__((ext_vector_type(2))); b2 r; r[0] = (__bf16)a; r[1] = (__bf16)b; return __builtin_bit_cast(unsigned, r); }
DI unsigned pkh(float a, float b) { f16x2 r; r[0] = (_Float16)a; r[1] = (_Float16)b; return __builtin_bit_cast(unsigned, r); }
DI u64 uni64(u64 v) { return (u64)(unsigned)__builtin_amdgcn_readfirstlane((unsigned)v) | ((u64)(unsigned)__builtin_amdgcn_readfirstlane((unsigned)(v >> 32)) << 32); }

DI unsigned tagbits(u64 ep, int j) { const unsigned n = ((unsigned)ep + 5 * j) & 15; return (n & 3) | ((n >> 2) << 16); }
typedef unsigned u32x8 __attribute__((ext_vector_type(8)));
typedef unsigned u32x16 __attribute__((ext_vector_type(16)));
struct CinIn { unsigned lw[5], go0, go1, fra, era, twa, pt, pp; };

DI int cin_pre(CinIn& in, int ct, int c, int lane, int blk, const u16* bo, char* ws, u64& ep, unsigned& tb1) {
  const u16 bov = bo[0];
  const int r = lane & 31, hh = lane >> 5;
  constexpr u64 FM[8] = {0x4184c48c0ull, 0x45a5ccce1ull, 0x59cad5102ull, 0x5debd5523ull, 0x1b08b2981ull, 0x1f29bada0ull, 0x4854d3a04ull, 0x4c75d3e25ull};
  const int q = lane & 3, gq = (lane >> 2) & 3;
  const u64 f0 = (q & 2) ? ((q & 1) ? FM[3] : FM[2]) : ((q & 1) ? FM[1] : FM[0]);
  const u64 f1 = (q & 2) ? ((q & 1) ? FM[7] : FM[6]) : ((q & 1) ? FM[5] : FM[4]);
  const u64 fm = (gq >> 1) ? f1 : f0;
  unsigned oa[7];
#pragma unroll
  for (int j = 0; j < 7; ++j) { oa[j] = IDX16 + (c * 16 + (lane >> 4) * 2 + (gq & 1)) * 52 + min((int)(fm >> (5 * j)) & 31, 25) * 2; asm volatile("" : "+v"(oa[j])); }
#pragma unroll
  for (int k = 0; k < 5; ++k) { const unsigned p = c * 312 + min(k * 64 + lane, 311), row = (p * 1366) >> 16; in.lw[k] = LAND + row * (ROWSTR - 768) + p * 16; asm volatile("" : "+v"(in.lw[k])); }
  const int ri = r < 25 ? r : 25;
  in.go0 = (c * 312 + lane) * 16; in.go1 = (c * 312 + min(256 + lane, 311)) * 16;
  in.era = LAND + ri * ROWSTR;
  in.fra = ((r < 25 || r == 28) ? in.era : ZROW) + hh * 16;
  in.twa = ct * 16;
  in.pt = OFFS + c * 2048 + (lane >> 4) * 64 + gq * 16; in.pp = q * 8;
  asm volatile("" : "+v"(in.go0), "+v"(in.go1), "+v"(in.fra), "+v"(in.era), "+v"(in.twa), "+v"(in.pt), "+v"(in.pp));
  if (c == 2 && lane < 48) *LDSP(u32x4, ZROW + lane * 16) = u32x4{0, 0, 0, 0};
  if (c == 3 && lane < 2) *LDSP(u32x4, CONSTROW + lane * 16) = u32x4{0x78007800u, 0x78007800u, 0x78007800u, 0x78007800u};
  const int role = c != 0 ? 0 : (blk >= 26 && blk < 52) ? 2 : (blk >= 52 && blk < 78) ? 3 : 0;
  unsigned g0 = hh * 2048 + min(r, 25) * 8, g1 = g0 + 4096, mk = r < 26 ? ~0u : 0u, cw = CELLS + hh * 512 + r * 4, so = (role == 2 ? blk : blk - 52) * 768 + lane * 16;
  asm volatile("" : "+v"(g0), "+v"(g1), "+v"(mk), "+v"(cw), "+v"(so));
  unsigned tba = TB + ct * 16, tb7 = TB + min(28672 + ct * 16, 31984), ow = OFFS + (c * 8 + (lane >> 4)) * 256 + (lane & 15) * 16, crow = CONSTROW;
  asm volatile("" : "+v"(tba), "+v"(tb7), "+v"(ow), "+v"(crow));
  if (c == 0)
    asm volatile(
      "v_mov_b32 v122, 61376\n"
      "s_waitcnt vmcnt(0)\n"
      "v_add_co_u32 v250, vcc, 1, v250\n"
      "s_nop 1\n"
      "v_addc_co_u32 v251, vcc, 0, v251, vcc\n"
      "s_mov_b64 exec, 1\n"
      "ds_write_b64 v122, v[250:251] offset:56\n"
      "s_mov_b64 exec, -1\n"
      "s_cmp_ge_u32 %[blk], 26\n"
      "s_cbranch_scc1 1f\n"
      "v_lshlrev_b32 v112, 16, v232\n"
      "v_lshlrev_b32 v113, 16, v233\n"
      "v_lshlrev_b32 v114, 16, v234\n"
      "v_lshlrev_b32 v115, 16, v235\n"
      "v_lshlrev_b32 v116, 16, v236\n"
      "v_lshlrev_b32 v117, 16, v237\n"
      "v_lshlrev_b32 v118, 16, v238\n"
      "v_and_b32 v232, 0xffff0000, v232\n"
      "v_and_b32 v233, 0xffff0000, v233\n"
      "v_and_b32 v234, 0xffff0000, v234\n"
      "v_and_b32 v235, 0xffff0000, v235\n"
      "v_and_b32 v236, 0xffff0000, v236\n"
      "v_and_b32 v237, 0xffff0000, v237\n"
      "v_and_b32 v238, 0xffff0000, v238\n"
      "v_mul_f32 v248, v112, v240\n"
      "v_mul_f32 v249, v232, v240\n"
      "v_fmac_f32 v248, v113, v241\n"
      "v_fmac_f32 v249, v233, v241\n"
      "v_fmac_f32 v248, v114, v242\n"
      "v_fmac_f32 v249, v234, v242\n"
      "v_fmac_f32 v248, v115, v243\n"
      "v_fmac_f32 v249, v235, v243\n"
      "v_fmac_f32 v248, v116, v244\n"
      "v_fmac_f32 v249, v236, v244\n"
      "v_fmac_f32 v248, v117, v245\n"
      "v_fmac_f32 v249, v237, v245\n"
      "v_fmac_f32 v248, v118, v246\n"
      "v_fmac_f32 v249, v238, v246\n"
      "v_mov_b32 v251, v250\n"
      "v_lshrrev_b32 v125, 5, v121\n"
      "v_permlane32_swap_b32 v248, v249\n"
      "v_lshl_add_u32 v125, v124, 1, v125\n"
      "s_lshl_b32 s57, %[blk], 8\n"
      "v_add_f32 v248, v248, v249\n"
      "v_lshl_add_u32 v125, v125, 3, s57\n"
      "v_mov_b32 v249, v248\n"
      "s_mov_b32 s54, 0x1fff\n"
      "s_mov_b32 s55, s54\n"
      "v_permlane16_swap_b32 v248, v249\n"
      "s_nop 0\n"
      "v_add_f32 v248, v248, v249\n"
      "s_nop 0\n"
      "v_cvt_pk_bf16_f32 v248, v248, v248\n"
      "s_nop 0\n"
      "v_and_b32 v250, 0xffff, v248\n"
      "s_mov_b64 exec, s[54:55]\n"
      "global_store_dwordx2 v125, v[250:251], %[wm3] sc1\n"
      "s_mov_b64 exec, -1\n"
      "1:\n"
      "s_sub_u32 s57, %[blk], 26\n"
      "s_cmp_ge_u32 s57, 26\n"
      "s_cbranch_scc1 3f\n"
      "v_or_b32 v232, v104, v232\n"
      "v_or_b32 v233, v105, v233\n"
      "v_or_b32 v234, v106, v234\n"
      "v_or_b32 v235, v107, v235\n"
      "v_or_b32 v236, v108, v236\n"
      "v_or_b32 v237, v109, v237\n"
      "v_or_b32 v238, v110, v238\n"
      "v_or_b32 v239, v111, v239\n"
      "3:\n"
      "s_sub_u32 s57, %[blk], 52\n"
      "s_cmp_ge_u32 s57, 26\n"
      "s_cbranch_scc1 2f\n"
      "v_add_u32 v240, 0, v121\n"
      "v_add_u32 v241, 64, v121\n"
      "v_add_u32 v242, 128, v121\n"
      "v_add_u32 v243, 192, v121\n"
      "v_add_u32 v244, 256, v121\n"
      "v_add_u32 v245, 320, v121\n"
      "v_min_u32 v245, 337, v245\n"
      "v_mul_u32_u24 v122, 5042, v240\n"
      "v_mul_u32_u24 v123, 5042, v241\n"
      "v_mul_u32_u24 v124, 5042, v242\n"
      "v_mul_u32_u24 v125, 5042, v243\n"
      "v_mul_u32_u24 v126, 5042, v244\n"
      "v_mul_u32_u24 v127, 5042, v245\n"
      "v_lshrrev_b32 v122, 16, v122\n"
      "v_lshrrev_b32 v123, 16, v123\n"
      "v_lshrrev_b32 v124, 16, v124\n"
      "v_lshrrev_b32 v125, 16, v125\n"
      "v_lshrrev_b32 v126, 16, v126\n"
      "v_lshrrev_b32 v127, 16, v127\n"
      "v_mul_u32_u24 v122, 24, v122\n"
      "v_mul_u32_u24 v123, 24, v123\n"
      "v_mul_u32_u24 v124, 24, v124\n"
      "v_mul_u32_u24 v125, 24, v125\n"
      "v_mul_u32_u24 v126, 24, v126\n"
      "v_mul_u32_u24 v127, 24, v127\n"
      "v_lshl_add_u32 v122, v240, 3, v122\n"
      "v_lshl_add_u32 v123, v241, 3, v123\n"
      "v_lshl_add_u32 v124, v242, 3, v124\n"
      "v_lshl_add_u32 v125, v243, 3, v125\n"
      "v_lshl_add_u32 v126, v244, 3, v126\n"
      "v_lshl_add_u32 v127, v245, 3, v127\n"
      "v_add_u32 v122, 100160, v122\n"
      "v_add_u32 v123, 100160, v123\n"
      "v_add_u32 v124, 100160, v124\n"
      "v_add_u32 v125, 100160, v125\n"
      "v_add_u32 v126, 100160, v126\n"
      "v_add_u32 v127, 100160, v127\n"
      "v_lshlrev_b32 v240, 16, v232\n"
      "v_lshlrev_b32 v241, 16, v233\n"
      "v_lshlrev_b32 v242, 16, v234\n"
      "v_lshlrev_b32 v243, 16, v235\n"
      "v_lshlrev_b32 v244, 16, v236\n"
      "v_lshlrev_b32 v245, 16, v237\n"
      "v_and_b32 v232, 0xffff0000, v232\n"
      "v_and_b32 v233, 0xffff0000, v233\n"
      "v_and_b32 v234, 0xffff0000, v234\n"
      "v_and_b32 v235, 0xffff0000, v235\n"
      "v_and_b32 v236, 0xffff0000, v236\n"
      "v_and_b32 v237, 0xffff0000, v237\n"
      "ds_write2_b32 v122, v240, v232 offset0:0 offset1:1\n"
      "ds_write2_b32 v123, v241, v233 offset0:0 offset1:1\n"
      "ds_write2_b32 v124, v242, v234 offset0:0 offset1:1\n"
      "ds_write2_b32 v125, v243, v235 offset0:0 offset1:1\n"
      "ds_write2_b32 v126, v244, v236 offset0:0 offset1:1\n"
      "ds_write2_b32 v127, v245, v237 offset0:0 offset1:1\n"
      "v_mov_b32 v122, 100268\n"
      "v_mov_b32 v123, 0\n"
      "s_mov_b64 exec, 1\n"
      "ds_write_b32 v122, v123\n"
      "s_mov_b32 exec_lo, 0\n"
      "s_mov_b32 exec_hi, 0xfffff000\n"
      "v_mov_b32 v112, 0x6c006c\n"
      "v_mov_b32 v113, 0x6c006c\n"
      "v_mov_b32 v114, 0x6c006c\n"
      "v_mov_b32 v115, 0x6c006c\n"
      "v_mov_b32 v116, 0x6c006c\n"
      "v_mov_b32 v117, 0x6c006c\n"
      "v_mov_b32 v118, 0x6c006c\n"
      "v_mov_b32 v119, 0x6c006c\n"
      "s_mov_b64 exec, -1\n"
      "2:\n"
      :: [blk] "s"(blk), [wm3] "s"(ws + WS_M3) : "v104", "v105", "v106", "v107", "v108", "v109", "v110", "v111", "v112", "v113", "v114", "v115", "v116", "v117", "v118", "v119", "v120", "v121", "v122", "v123", "v124", "v125", "v126", "v127", "v232", "v233", "v234", "v235", "v236", "v237", "v238", "v239", "v240", "v241", "v242", "v243", "v244", "v245", "v246", "v247", "v248", "v249", "v250", "v251", "s54", "s55", "s57", "vcc", "scc", "memory");
  asm volatile("s_waitcnt lgkmcnt(0)\n s_barrier" ::: "memory");
  if (c == 1 && lane == 0) *LDSP(float, FLAGS + 52) = bf1(bov);
  { u64 t; asm volatile("s_memtime %0\n s_waitcnt lgkmcnt(0)" : "=s"(t) :: "memory"); tb1 = (unsigned)t; }
  unsigned epl, eph;
  int ex;
  if (role == 0)
    asm volatile(
      "ds_read_b128 v[200:203], %[tba] offset:0\n"
      "ds_read_b128 v[204:207], %[tba] offset:4096\n"
      "ds_read_b128 v[208:211], %[tba] offset:8192\n"
      "ds_read_b128 v[212:215], %[tba] offset:12288\n"
      "ds_read_b128 v[216:219], %[tba] offset:16384\n"
      "ds_read_b128 v[220:223], %[tba] offset:20480\n"
      "ds_read_b128 v[224:227], %[tba] offset:24576\n"
      "ds_read_b128 v[228:231], %[tb7]\n"
      "v_mov_b32 v182, 61376\n"
      "v_mov_b32 v183, 1\n"
      "s_mov_b32 s60, 0x7fff7fff\n"
      "s_mov_b32 s62, 0x10001\n"
      "s_mov_b32 s63, s62\n"
      "s_waitcnt lgkmcnt(6)\n"
      "v_pk_maximum3_f16 v132, v200, v204, v200\n"
      "v_pk_minimum3_f16 v136, v200, v204, v200\n"
      "v_pk_maximum3_f16 v133, v201, v205, v201\n"
      "v_pk_minimum3_f16 v137, v201, v205, v201\n"
      "v_pk_maximum3_f16 v134, v202, v206, v202\n"
      "v_pk_minimum3_f16 v138, v202, v206, v202\n"
      "v_pk_maximum3_f16 v135, v203, v207, v203\n"
      "v_pk_minimum3_f16 v139, v203, v207, v203\n"
      "s_waitcnt lgkmcnt(4)\n"
      "v_pk_maximum3_f16 v132, v208, v212, v132\n"
      "v_pk_minimum3_f16 v136, v208, v212, v136\n"
      "v_pk_maximum3_f16 v133, v209, v213, v133\n"
      "v_pk_minimum3_f16 v137, v209, v213, v137\n"
      "v_pk_maximum3_f16 v134, v210, v214, v134\n"
      "v_pk_minimum3_f16 v138, v210, v214, v138\n"
      "v_pk_maximum3_f16 v135, v211, v215, v135\n"
      "v_pk_minimum3_f16 v139, v211, v215, v139\n"
      "ds_read_u16 v164, %[oa0] offset:0\n"
      "ds_read_u16 v165, %[oa1] offset:0\n"
      "ds_read_u16 v166, %[oa2] offset:0\n"
      "ds_read_u16 v167, %[oa3] offset:0\n"
      "ds_read_u16 v168, %[oa4] offset:0\n"
      "ds_read_u16 v169, %[oa5] offset:0\n"
      "ds_read_u16 v170, %[oa6] offset:0\n"
      "ds_read_u16 v171, %[oa0] offset:416\n"
      "ds_read_u16 v172, %[oa1] offset:416\n"
      "ds_read_u16 v173, %[oa2] offset:416\n"
      "ds_read_u16 v174, %[oa3] offset:416\n"
      "ds_read_u16 v175, %[oa4] offset:416\n"
      "ds_read_u16 v176, %[oa5] offset:416\n"
      "ds_read_u16 v177, %[oa6] offset:416\n"
      "ds_read_b64 v[130:131], v182 offset:56\n"
      "s_waitcnt lgkmcnt(15)\n"
      "v_pk_maximum3_f16 v132, v216, v220, v132\n"
      "v_pk_minimum3_f16 v136, v216, v220, v136\n"
      "v_pk_maximum3_f16 v133, v217, v221, v133\n"
      "v_pk_minimum3_f16 v137, v217, v221, v137\n"
      "v_pk_maximum3_f16 v134, v218, v222, v134\n"
      "v_pk_minimum3_f16 v138, v218, v222, v138\n"
      "v_pk_maximum3_f16 v135, v219, v223, v135\n"
      "v_pk_minimum3_f16 v139, v219, v223, v139\n"
      "v_pk_maximum3_f16 v132, v224, v228, v132\n"
      "v_pk_minimum3_f16 v136, v224, v228, v136\n"
      "v_pk_maximum3_f16 v133, v225, v229, v133\n"
      "v_pk_minimum3_f16 v137, v225, v229, v137\n"
      "v_pk_maximum3_f16 v134, v226, v230, v134\n"
      "v_pk_minimum3_f16 v138, v226, v230, v138\n"
      "v_pk_maximum3_f16 v135, v227, v231, v135\n"
      "v_pk_minimum3_f16 v139, v227, v231, v139\n"
      "v_and_b32 v132, s60, v132\n"
      "v_and_b32 v133, s60, v133\n"
      "v_and_b32 v134, s60, v134\n"
      "v_and_b32 v135, s60, v135\n"
      "v_and_b32 v136, s60, v136\n"
      "v_and_b32 v137, s60, v137\n"
      "v_and_b32 v138, s60, v138\n"
      "v_and_b32 v139, s60, v139\n"
      "v_pk_max_u16 v178, v132, v136\n"
      "v_pk_max_u16 v179, v133, v137\n"
      "v_pk_max_u16 v180, v134, v138\n"
      "v_pk_max_u16 v181, v135, v139\n"
      "v_pk_max_u16 v178, v178, v179\n"
      "v_pk_max_u16 v180, v180, v181\n"
      "s_waitcnt lgkmcnt(0)\n"
      "v_pk_max_u16 v178, v178, v180\n"
      "v_cndmask_b32_e64 v167, v167, %[cr], %[qm]\n"
      "v_max_u16_sdwa v178, v178, v178 dst_sel:DWORD dst_unused:UNUSED_PAD src0_sel:WORD_0 src1_sel:WORD_1\n"
      "v_lshl_or_b32 v164, v165, 16, v164\n"
      "v_lshl_or_b32 v168, v169, 16, v168\n"
      "v_max_u32_dpp v178, v178, v178 quad_perm:[1,0,3,2] row_mask:0xf bank_mask:0xf\n"
      "v_lshl_or_b32 v165, v167, 16, v166\n"
      "v_cndmask_b32_e64 v174, v174, %[cr], %[qm]\n"
      "v_max_u32_dpp v178, v178, v178 quad_perm:[2,3,0,1] row_mask:0xf bank_mask:0xf\n"
      "v_lshl_or_b32 v171, v172, 16, v171\n"
      "v_lshl_or_b32 v175, v176, 16, v175\n"
      "v_max_u32_dpp v178, v178, v178 row_half_mirror row_mask:0xf bank_mask:0xf\n"
      "v_lshl_or_b32 v172, v174, 16, v173\n"
      "v_mov_b32 v166, v168\n"
      "v_max_u32_dpp v178, v178, v178 row_mirror row_mask:0xf bank_mask:0xf\n"
      "v_mov_b32 v167, v170\n"
      "v_mov_b32 v168, v171\n"
      "s_mov_b64 exec, s[62:63]\n"
      "ds_max_u32 v182, v178 offset:32\n"
      "s_mov_b64 exec, 1\n"
      "ds_add_u32 v182, v183 offset:36\n"
      "s_mov_b64 exec, -1\n"
      "v_mov_b32 v169, v172\n"
      "v_mov_b32 v170, v175\n"
      "v_mov_b32 v171, v177\n"
      "s_waitcnt vmcnt(0)\n"
      "ds_write_b128 %[ow], v[164:167]\n"
      "ds_write_b128 %[ow], v[168:171] offset:1024\n"
      "v_readfirstlane_b32 %[epl], v130\n"
      "v_readfirstlane_b32 %[eph], v131\n"
      "s_branch 2f\n"
      "1:\n"
      "s_sleep 1\n"
      "2:\n"
      "ds_read_b64 v[178:179], v182 offset:32\n"
      "s_waitcnt lgkmcnt(0)\n"
      "v_cmp_gt_u32 vcc, 4, v179\n"
      "s_cbranch_vccnz 1b\n"
      "v_readfirstlane_b32 s60, v178\n"
      "s_lshr_b32 s61, s60, 7\n"
      "s_max_u32 s61, s61, 22\n"
      "s_sub_u32 s62, s61, 134\n"
      "s_cmp_lg_u32 s60, 0\n"
      "s_cselect_b32 %[ex], s62, 0\n"
      : [ex] "=&s"(ex), [epl] "=&s"(epl), [eph] "=&s"(eph)
      : [tba] "v"(tba), [tb7] "v"(tb7), [ow] "v"(ow), [oa0] "v"(oa[0]), [oa1] "v"(oa[1]), [oa2] "v"(oa[2]), [oa3] "v"(oa[3]),
        [oa4] "v"(oa[4]), [oa5] "v"(oa[5]), [oa6] "v"(oa[6]), [cr] "v"(crow), [qm] "s"(0xccccccccccccccccull)
      : "v130", "v131", "v132", "v133", "v134", "v135", "v136", "v137", "v138", "v139", "v140", "v141", "v142", "v143", "v144", "v145", "v146", "v147", "v148", "v149", "v150", "v151", "v152", "v153", "v154", "v155", "v156", "v157", "v158", "v159", "v160", "v161", "v162", "v163", "v164", "v165", "v166", "v167", "v168", "v169", "v170", "v171", "v172", "v173", "v174", "v175", "v176", "v177", "v178", "v179", "v180", "v181", "v182", "v183", "v200", "v201", "v202", "v203", "v204", "v205", "v206", "v207", "v208", "v209", "v210", "v211", "v212", "v213", "v214", "v215", "v216", "v217", "v218", "v219", "v220", "v221", "v222", "v223", "v224", "v225", "v226", "v227", "v228", "v229", "v230", "v231", "s60", "s61", "s62", "s63", "s72", "s73", "s74", "s75", "vcc", "scc", "memory");
  else if (role == 2)
    asm volatile(
      "s_setprio 3\n"
      "v_mov_b32 v72, 0\n"
      "v_mov_b32 v74, 0\n"
      "v_mov_b32 v76, 0\n"
      "v_mov_b32 v78, 0\n"
      "v_mov_b32 v80, 0\n"
      "v_mov_b32 v82, 0\n"
      "ds_read_b128 v[200:203], %[tba] offset:0\n"
      "ds_read_b128 v[204:207], %[tba] offset:4096\n"
      "ds_read_b128 v[208:211], %[tba] offset:8192\n"
      "ds_read_b128 v[212:215], %[tba] offset:12288\n"
      "ds_read_b128 v[216:219], %[tba] offset:16384\n"
      "ds_read_b128 v[220:223], %[tba] offset:20480\n"
      "ds_read_b128 v[224:227], %[tba] offset:24576\n"
      "ds_read_b128 v[228:231], %[tb7]\n"
      "v_mov_b32 v182, 61376\n"
      "v_mov_b32 v183, 1\n"
      "s_mov_b32 s60, 0x7fff7fff\n"
      "s_mov_b32 s62, 0x10001\n"
      "s_mov_b32 s63, s62\n"
      "s_waitcnt lgkmcnt(6)\n"
      "v_pk_maximum3_f16 v132, v200, v204, v200\n"
      "v_pk_minimum3_f16 v136, v200, v204, v200\n"
      "v_pk_maximum3_f16 v133, v201, v205, v201\n"
      "v_pk_minimum3_f16 v137, v201, v205, v201\n"
      "v_pk_maximum3_f16 v134, v202, v206, v202\n"
      "v_pk_minimum3_f16 v138, v202, v206, v202\n"
      "v_pk_maximum3_f16 v135, v203, v207, v203\n"
      "v_pk_minimum3_f16 v139, v203, v207, v203\n"
      "s_waitcnt lgkmcnt(4)\n"
      "v_pk_maximum3_f16 v132, v208, v212, v132\n"
      "v_pk_minimum3_f16 v136, v208, v212, v136\n"
      "v_pk_maximum3_f16 v133, v209, v213, v133\n"
      "v_pk_minimum3_f16 v137, v209, v213, v137\n"
      "v_pk_maximum3_f16 v134, v210, v214, v134\n"
      "v_pk_minimum3_f16 v138, v210, v214, v138\n"
      "v_pk_maximum3_f16 v135, v211, v215, v135\n"
      "v_pk_minimum3_f16 v139, v211, v215, v139\n"
      "ds_read_u16 v164, %[oa0] offset:0\n"
      "ds_read_u16 v165, %[oa1] offset:0\n"
      "ds_read_u16 v166, %[oa2] offset:0\n"
      "ds_read_u16 v167, %[oa3] offset:0\n"
      "ds_read_u16 v168, %[oa4] offset:0\n"
      "ds_read_u16 v169, %[oa5] offset:0\n"
      "ds_read_u16 v170, %[oa6] offset:0\n"
      "ds_read_u16 v171, %[oa0] offset:416\n"
      "ds_read_u16 v172, %[oa1] offset:416\n"
      "ds_read_u16 v173, %[oa2] offset:416\n"
      "ds_read_u16 v174, %[oa3] offset:416\n"
      "ds_read_u16 v175, %[oa4] offset:416\n"
      "ds_read_u16 v176, %[oa5] offset:416\n"
      "ds_read_u16 v177, %[oa6] offset:416\n"
      "ds_read_b64 v[130:131], v182 offset:56\n"
      "s_waitcnt lgkmcnt(15)\n"
      "v_pk_maximum3_f16 v132, v216, v220, v132\n"
      "v_pk_minimum3_f16 v136, v216, v220, v136\n"
      "v_pk_maximum3_f16 v133, v217, v221, v133\n"
      "v_pk_minimum3_f16 v137, v217, v221, v137\n"
      "v_pk_maximum3_f16 v134, v218, v222, v134\n"
      "v_pk_minimum3_f16 v138, v218, v222, v138\n"
      "v_pk_maximum3_f16 v135, v219, v223, v135\n"
      "v_pk_minimum3_f16 v139, v219, v223, v139\n"
      "v_pk_maximum3_f16 v132, v224, v228, v132\n"
      "v_pk_minimum3_f16 v136, v224, v228, v136\n"
      "v_pk_maximum3_f16 v133, v225, v229, v133\n"
      "v_pk_minimum3_f16 v137, v225, v229, v137\n"
      "v_pk_maximum3_f16 v134, v226, v230, v134\n"
      "v_pk_minimum3_f16 v138, v226, v230, v138\n"
      "v_pk_maximum3_f16 v135, v227, v231, v135\n"
      "v_pk_minimum3_f16 v139, v227, v231, v139\n"
      "v_and_b32 v132, s60, v132\n"
      "v_and_b32 v133, s60, v133\n"
      "v_and_b32 v134, s60, v134\n"
      "v_and_b32 v135, s60, v135\n"
      "v_and_b32 v136, s60, v136\n"
      "v_and_b32 v137, s60, v137\n"
      "v_and_b32 v138, s60, v138\n"
      "v_and_b32 v139, s60, v139\n"
      "v_pk_max_u16 v178, v132, v136\n"
      "v_pk_max_u16 v179, v133, v137\n"
      "v_pk_max_u16 v180, v134, v138\n"
      "v_pk_max_u16 v181, v135, v139\n"
      "v_pk_max_u16 v178, v178, v179\n"
      "v_pk_max_u16 v180, v180, v181\n"
      "s_waitcnt lgkmcnt(0)\n"
      "v_pk_max_u16 v178, v178, v180\n"
      "v_cndmask_b32_e64 v167, v167, %[cr], %[qm]\n"
      "v_max_u16_sdwa v178, v178, v178 dst_sel:DWORD dst_unused:UNUSED_PAD src0_sel:WORD_0 src1_sel:WORD_1\n"
      "v_lshl_or_b32 v164, v165, 16, v164\n"
      "v_lshl_or_b32 v168, v169, 16, v168\n"
      "v_max_u32_dpp v178, v178, v178 quad_perm:[1,0,3,2] row_mask:0xf bank_mask:0xf\n"
      "v_lshl_or_b32 v165, v167, 16, v166\n"
      "v_cndmask_b32_e64 v174, v174, %[cr], %[qm]\n"
      "v_max_u32_dpp v178, v178, v178 quad_perm:[2,3,0,1] row_mask:0xf bank_mask:0xf\n"
      "v_lshl_or_b32 v171, v172, 16, v171\n"
      "v_lshl_or_b32 v175, v176, 16, v175\n"
      "v_max_u32_dpp v178, v178, v178 row_half_mirror row_mask:0xf bank_mask:0xf\n"
      "v_lshl_or_b32 v172, v174, 16, v173\n"
      "v_mov_b32 v166, v168\n"
      "v_max_u32_dpp v178, v178, v178 row_mirror row_mask:0xf bank_mask:0xf\n"
      "v_mov_b32 v167, v170\n"
      "v_mov_b32 v168, v171\n"
      "s_mov_b64 exec, s[62:63]\n"
      "ds_max_u32 v182, v178 offset:32\n"
      "s_mov_b64 exec, 1\n"
      "ds_add_u32 v182, v183 offset:36\n"
      "s_mov_b64 exec, -1\n"
      "global_load_dwordx2 v[52:53], %[g0], %[wm3] offset:0 sc1\n"
      "global_load_dwordx2 v[54:55], %[g0], %[wm3] offset:256 sc1\n"
      "global_load_dwordx2 v[56:57], %[g0], %[wm3] offset:512 sc1\n"
      "global_load_dwordx2 v[58:59], %[g0], %[wm3] offset:768 sc1\n"
      "global_load_dwordx2 v[60:61], %[g0], %[wm3] offset:1024 sc1\n"
      "global_load_dwordx2 v[62:63], %[g0], %[wm3] offset:1280 sc1\n"
      "global_load_dwordx2 v[64:65], %[g0], %[wm3] offset:1536 sc1\n"
      "global_load_dwordx2 v[66:67], %[g0], %[wm3] offset:1792 sc1\n"
      "global_load_dwordx2 v[68:69], %[g1], %[wm3] offset:0 sc1\n"
      "global_load_dwordx2 v[70:71], %[g1], %[wm3] offset:256 sc1\n"
      "s_mov_b32 exec_hi, 0\n"
      "global_load_dwordx2 v[72:73], %[g1], %[wm3] offset:512 sc1\n"
      "global_load_dwordx2 v[74:75], %[g1], %[wm3] offset:768 sc1\n"
      "global_load_dwordx2 v[76:77], %[g1], %[wm3] offset:1024 sc1\n"
      "global_load_dwordx2 v[78:79], %[g1], %[wm3] offset:1280 sc1\n"
      "global_load_dwordx2 v[80:81], %[g1], %[wm3] offset:1536 sc1\n"
      "global_load_dwordx2 v[82:83], %[g1], %[wm3] offset:1792 sc1\n"
      "s_mov_b32 exec_hi, -1\n"
      "v_mov_b32 v169, v172\n"
      "v_mov_b32 v170, v175\n"
      "v_mov_b32 v171, v177\n"
      "ds_write_b128 %[ow], v[164:167]\n"
      "ds_write_b128 %[ow], v[168:171] offset:1024\n"
      "v_readfirstlane_b32 %[epl], v130\n"
      "v_readfirstlane_b32 %[eph], v131\n"
      "s_branch 2f\n"
      "1:\n"
      "s_sleep 1\n"
      "2:\n"
      "ds_read_b64 v[178:179], v182 offset:32\n"
      "s_waitcnt lgkmcnt(0)\n"
      "v_cmp_gt_u32 vcc, 4, v179\n"
      "s_cbranch_vccnz 1b\n"
      "v_readfirstlane_b32 s60, v178\n"
      "s_lshr_b32 s61, s60, 7\n"
      "s_max_u32 s61, s61, 22\n"
      "s_sub_u32 s62, s61, 134\n"
      "s_cmp_lg_u32 s60, 0\n"
      "s_cselect_b32 %[ex], s62, 0\n"
      "v_mov_b32 v104, 100160\n"
      "v_add_u32_sdwa v36, v112, v104 dst_sel:DWORD dst_unused:UNUSED_PAD src0_sel:WORD_0 src1_sel:DWORD\n"
      "v_add_u32_sdwa v37, v112, v104 dst_sel:DWORD dst_unused:UNUSED_PAD src0_sel:WORD_1 src1_sel:DWORD\n"
      "v_add_u32_sdwa v38, v113, v104 dst_sel:DWORD dst_unused:UNUSED_PAD src0_sel:WORD_0 src1_sel:DWORD\n"
      "v_add_u32_sdwa v39, v113, v104 dst_sel:DWORD dst_unused:UNUSED_PAD src0_sel:WORD_1 src1_sel:DWORD\n"
      "v_add_u32_sdwa v40, v114, v104 dst_sel:DWORD dst_unused:UNUSED_PAD src0_sel:WORD_0 src1_sel:DWORD\n"
      "v_add_u32_sdwa v41, v114, v104 dst_sel:DWORD dst_unused:UNUSED_PAD src0_sel:WORD_1 src1_sel:DWORD\n"
      "v_add_u32_sdwa v42, v115, v104 dst_sel:DWORD dst_unused:UNUSED_PAD src0_sel:WORD_0 src1_sel:DWORD\n"
      "v_add_u32_sdwa v43, v115, v104 dst_sel:DWORD dst_unused:UNUSED_PAD src0_sel:WORD_1 src1_sel:DWORD\n"
      "v_add_u32_sdwa v44, v116, v104 dst_sel:DWORD dst_unused:UNUSED_PAD src0_sel:WORD_0 src1_sel:DWORD\n"
      "v_add_u32_sdwa v45, v116, v104 dst_sel:DWORD dst_unused:UNUSED_PAD src0_sel:WORD_1 src1_sel:DWORD\n"
      "v_add_u32_sdwa v46, v117, v104 dst_sel:DWORD dst_unused:UNUSED_PAD src0_sel:WORD_0 src1_sel:DWORD\n"
      "v_add_u32_sdwa v47, v117, v104 dst_sel:DWORD dst_unused:UNUSED_PAD src0_sel:WORD_1 src1_sel:DWORD\n"
      "v_add_u32_sdwa v48, v118, v104 dst_sel:DWORD dst_unused:UNUSED_PAD src0_sel:WORD_0 src1_sel:DWORD\n"
      "v_add_u32_sdwa v49, v118, v104 dst_sel:DWORD dst_unused:UNUSED_PAD src0_sel:WORD_1 src1_sel:DWORD\n"
      "v_add_u32_sdwa v50, v119, v104 dst_sel:DWORD dst_unused:UNUSED_PAD src0_sel:WORD_0 src1_sel:DWORD\n"
      "v_add_u32_sdwa v51, v119, v104 dst_sel:DWORD dst_unused:UNUSED_PAD src0_sel:WORD_1 src1_sel:DWORD\n"
      "s_add_u32 s64, %[epl], 0\n"
      "s_bfe_u32 s68, s64, 0x20002\n"
      "s_and_b32 s64, s64, 3\n"
      "s_lshl_b32 s68, s68, 16\n"
      "s_or_b32 s64, s64, s68\n"
      "s_add_u32 s65, %[epl], 5\n"
      "s_bfe_u32 s68, s65, 0x20002\n"
      "s_and_b32 s65, s65, 3\n"
      "s_lshl_b32 s68, s68, 16\n"
      "s_or_b32 s65, s65, s68\n"
      "s_add_u32 s66, %[epl], 10\n"
      "s_bfe_u32 s68, s66, 0x20002\n"
      "s_and_b32 s66, s66, 3\n"
      "s_lshl_b32 s68, s68, 16\n"
      "s_or_b32 s66, s66, s68\n"
      "s_add_u32 s67, %[epl], 15\n"
      "s_bfe_u32 s68, s67, 0x20002\n"
      "s_and_b32 s67, s67, 3\n"
      "s_lshl_b32 s68, s68, 16\n"
      "s_or_b32 s67, s67, s68\n"
      "s_lshl_b32 s68, %[ex], 1\n"
      "v_mov_b32 v100, %[ex]\n"
      "v_mov_b32 v101, s68\n"
      "s_sub_u32 s68, -15, %[ex]\n"
      "v_cndmask_b32_e64 v100, v100, v101, %[ql]\n"
      "v_ldexp_f32 v101, v120, s68\n"
      "v_mov_b32 v102, 100272\n"
      "s_mov_b32 s69, 0x40000\n"
      "s_mov_b32 s70, 0x05040100\n"
      "5:\n"
      "s_waitcnt vmcnt(0)\n"
      "v_xor_b32 v84, %[epl], v53\n"
      "v_xor_b32 v85, %[epl], v55\n"
      "v_bitop3_b32 v84, v57, %[epl], v84 bitop3:0xbe\n"
      "v_bitop3_b32 v85, v59, %[epl], v85 bitop3:0xbe\n"
      "v_bitop3_b32 v84, v61, %[epl], v84 bitop3:0xbe\n"
      "v_bitop3_b32 v85, v63, %[epl], v85 bitop3:0xbe\n"
      "v_bitop3_b32 v84, v65, %[epl], v84 bitop3:0xbe\n"
      "v_bitop3_b32 v85, v67, %[epl], v85 bitop3:0xbe\n"
      "v_bitop3_b32 v84, v69, %[epl], v84 bitop3:0xbe\n"
      "v_bitop3_b32 v85, v71, %[epl], v85 bitop3:0xbe\n"
      "s_mov_b32 exec_hi, 0\n"
      "v_bitop3_b32 v84, v73, %[epl], v84 bitop3:0xbe\n"
      "v_bitop3_b32 v85, v75, %[epl], v85 bitop3:0xbe\n"
      "v_bitop3_b32 v84, v77, %[epl], v84 bitop3:0xbe\n"
      "v_bitop3_b32 v85, v79, %[epl], v85 bitop3:0xbe\n"
      "v_bitop3_b32 v84, v81, %[epl], v84 bitop3:0xbe\n"
      "v_bitop3_b32 v85, v83, %[epl], v85 bitop3:0xbe\n"
      "s_mov_b32 exec_hi, -1\n"
      "v_or_b32 v84, v84, v85\n"
      "s_nop 0\n"
      "v_cmp_ne_u32 vcc, 0, v84\n"
      "s_cbranch_vccz 6f\n"
      "global_load_dwordx2 v[52:53], %[g0], %[wm3] offset:0 sc1\n"
      "global_load_dwordx2 v[54:55], %[g0], %[wm3] offset:256 sc1\n"
      "global_load_dwordx2 v[56:57], %[g0], %[wm3] offset:512 sc1\n"
      "global_load_dwordx2 v[58:59], %[g0], %[wm3] offset:768 sc1\n"
      "global_load_dwordx2 v[60:61], %[g0], %[wm3] offset:1024 sc1\n"
      "global_load_dwordx2 v[62:63], %[g0], %[wm3] offset:1280 sc1\n"
      "global_load_dwordx2 v[64:65], %[g0], %[wm3] offset:1536 sc1\n"
      "global_load_dwordx2 v[66:67], %[g0], %[wm3] offset:1792 sc1\n"
      "global_load_dwordx2 v[68:69], %[g1], %[wm3] offset:0 sc1\n"
      "global_load_dwordx2 v[70:71], %[g1], %[wm3] offset:256 sc1\n"
      "s_mov_b32 exec_hi, 0\n"
      "global_load_dwordx2 v[72:73], %[g1], %[wm3] offset:512 sc1\n"
      "global_load_dwordx2 v[74:75], %[g1], %[wm3] offset:768 sc1\n"
      "global_load_dwordx2 v[76:77], %[g1], %[wm3] offset:1024 sc1\n"
      "global_load_dwordx2 v[78:79], %[g1], %[wm3] offset:1280 sc1\n"
      "global_load_dwordx2 v[80:81], %[g1], %[wm3] offset:1536 sc1\n"
      "global_load_dwordx2 v[82:83], %[g1], %[wm3] offset:1792 sc1\n"
      "s_mov_b32 exec_hi, -1\n"
      "s_sub_u32 s69, s69, 1\n"
      "s_cmp_lg_u32 s69, 0\n"
      "s_cbranch_scc1 5b\n"
      "s_waitcnt vmcnt(0)\n"
      "6:\n"
      "v_perm_b32 v52, v54, v52, s70\n"
      "v_perm_b32 v56, v58, v56, s70\n"
      "v_perm_b32 v60, v62, v60, s70\n"
      "v_perm_b32 v64, v66, v64, s70\n"
      "v_perm_b32 v68, v70, v68, s70\n"
      "v_perm_b32 v72, v74, v72, s70\n"
      "v_perm_b32 v76, v78, v76, s70\n"
      "v_perm_b32 v80, v82, v80, s70\n"
      "v_and_or_b32 v240, v52, %[mk], v240\n"
      "v_and_or_b32 v241, v56, %[mk], v241\n"
      "v_and_or_b32 v242, v60, %[mk], v242\n"
      "v_and_or_b32 v243, v64, %[mk], v243\n"
      "v_and_or_b32 v244, v68, %[mk], v244\n"
      "v_and_or_b32 v245, v72, %[mk], v245\n"
      "v_and_or_b32 v246, v76, %[mk], v246\n"
      "v_and_or_b32 v247, v80, %[mk], v247\n"
      "s_nop 1\n"
      "v_mfma_f32_32x32x16_bf16 v[84:99], v[232:235], v[240:243], 0\n"
      "v_mfma_f32_32x32x16_bf16 v[84:99], v[236:239], v[244:247], v[84:99]\n"
      "s_nop 11\n"
      "ds_write_b32 %[cw], v84 offset:0\n"
      "ds_write_b32 %[cw], v85 offset:128\n"
      "ds_write_b32 %[cw], v86 offset:256\n"
      "ds_write_b32 %[cw], v87 offset:384\n"
      "ds_write_b32 %[cw], v88 offset:1024\n"
      "ds_write_b32 %[cw], v89 offset:1152\n"
      "ds_write_b32 %[cw], v90 offset:1280\n"
      "ds_write_b32 %[cw], v91 offset:1408\n"
      "ds_write_b32 %[cw], v92 offset:2048\n"
      "ds_write_b32 %[cw], v93 offset:2176\n"
      "ds_write_b32 %[cw], v94 offset:2304\n"
      "ds_write_b32 %[cw], v95 offset:2432\n"
      "ds_write_b32 %[cw], v96 offset:3072\n"
      "ds_write_b32 %[cw], v97 offset:3200\n"
      "s_mov_b64 exec, 1\n"
      "ds_write_b32 v102, v101\n"
      "s_mov_b64 exec, -1\n"
      "ds_read_b32 v84, v36\n"
      "ds_read_b32 v85, v37\n"
      "ds_read_b32 v86, v38\n"
      "ds_read_b32 v87, v39\n"
      "ds_read_b32 v88, v40\n"
      "ds_read_b32 v89, v41\n"
      "ds_read_b32 v90, v42\n"
      "ds_read_b32 v91, v43\n"
      "ds_read_b32 v92, v44\n"
      "ds_read_b32 v93, v45\n"
      "ds_read_b32 v94, v46\n"
      "ds_read_b32 v95, v47\n"
      "ds_read_b32 v96, v48\n"
      "ds_read_b32 v97, v49\n"
      "ds_read_b32 v98, v50\n"
      "ds_read_b32 v99, v51\n"
      "s_mov_b64 s[72:73], %[wsf]\n"
      "s_and_b32 s73, s73, 0xffff\n"
      "s_mov_b32 s74, 39936\n"
      "s_mov_b32 s75, 0x20000\n"
      "s_mov_b32 s69, 0x20002\n"
      "s_mov_b32 s70, 0xfffcfffc\n"
      "s_waitcnt lgkmcnt(8)\n"
      "v_add_f32 v104, v84, v85\n"
      "v_add_f32 v105, v86, v87\n"
      "v_add_f32 v106, v88, v89\n"
      "v_add_f32 v107, v90, v91\n"
      "s_waitcnt lgkmcnt(0)\n"
      "v_add_f32 v108, v92, v93\n"
      "v_add_f32 v109, v94, v95\n"
      "v_add_f32 v110, v96, v97\n"
      "v_add_f32 v111, v98, v99\n"
      "v_max3_f32 v84, |v104|, |v105|, |v106|\n"
      "v_max3_f32 v85, |v108|, |v109|, |v110|\n"
      "v_max3_f32 v84, |v107|, |v111|, v84\n"
      "v_mov_b32 v87, 0xfffff000\n"
      "v_max_f32 v84, v84, v85\n"
      "s_nop 0\n"
      "v_frexp_exp_i32_f32 v85, v84\n"
      "v_cmp_lt_f32 vcc, 0, v84\n"
      "v_add_u32 v85, v85, v100\n"
      "s_nop 1\n"
      "v_cndmask_b32 v85, v87, v85, vcc\n"
      "s_nop 1\n"
      "v_max_i32_dpp v85, v85, v85 quad_perm:[1,0,3,2] row_mask:0xf bank_mask:0xf\n"
      "s_nop 1\n"
      "v_max_i32_dpp v85, v85, v85 quad_perm:[2,3,0,1] row_mask:0xf bank_mask:0xf\n"
      "s_nop 1\n"
      "v_max_i32_dpp v85, v85, v85 row_half_mirror row_mask:0xf bank_mask:0xf\n"
      "s_nop 1\n"
      "v_max_i32_dpp v85, v85, v85 row_mirror row_mask:0xf bank_mask:0xf\n"
      "s_nop 1\n"
      "v_max_i32_dpp v85, v85, v85 row_bcast:15 row_mask:0xa bank_mask:0xf\n"
      "s_nop 1\n"
      "v_max_i32_dpp v85, v85, v85 row_bcast:31 row_mask:0xc bank_mask:0xf\n"
      "s_nop 1\n"
      "v_readlane_b32 s71, v85, 63\n"
      "s_sub_u32 s71, s71, 8\n"
      "s_and_b32 s71, s71, -8\n"
      "v_subrev_u32 v100, s71, v100\n"
      "s_and_b32 s71, s71, 0x3fff\n"
      "s_lshl_b32 s71, s71, 2\n"
      "v_ldexp_f32 v104, v104, v100\n"
      "v_ldexp_f32 v105, v105, v100\n"
      "v_ldexp_f32 v106, v106, v100\n"
      "v_ldexp_f32 v107, v107, v100\n"
      "v_ldexp_f32 v108, v108, v100\n"
      "v_ldexp_f32 v109, v109, v100\n"
      "v_ldexp_f32 v110, v110, v100\n"
      "v_ldexp_f32 v111, v111, v100\n"
      "v_cvt_pk_f16_f32 v88, v104, v105\n"
      "v_cvt_pk_f16_f32 v89, v106, v107\n"
      "v_cvt_pk_f16_f32 v90, v108, v109\n"
      "v_cvt_pk_f16_f32 v91, v110, v111\n"
      "v_add_u32 v88, s69, v88\n"
      "v_add_u32 v89, s69, v89\n"
      "v_add_u32 v90, s69, v90\n"
      "v_add_u32 v91, s69, v91\n"
      "s_mov_b64 exec, %[el]\n"
      "v_mov_b32 v91, s71\n"
      "s_mov_b64 exec, -1\n"
      "v_and_b32 v88, s70, v88\n"
      "v_and_b32 v89, s70, v89\n"
      "v_and_b32 v90, s70, v90\n"
      "v_and_b32 v91, s70, v91\n"
      "v_or_b32 v88, s64, v88\n"
      "v_or_b32 v89, s65, v89\n"
      "v_or_b32 v90, s66, v90\n"
      "v_or_b32 v91, s67, v91\n"
      "s_mov_b32 exec_hi, 0xffff\n"
      "buffer_store_dwordx4 v[88:91], %[so], s[72:75], 0 offen sc1\n"
      "s_mov_b32 exec_hi, -1\n"
      "s_nop 1\n"
      "s_setprio 0\n"
      : [ex] "=&s"(ex), [epl] "=&s"(epl), [eph] "=&s"(eph)
      : [tba] "v"(tba), [tb7] "v"(tb7), [ow] "v"(ow), [oa0] "v"(oa[0]), [oa1] "v"(oa[1]), [oa2] "v"(oa[2]), [oa3] "v"(oa[3]),
        [oa4] "v"(oa[4]), [oa5] "v"(oa[5]), [oa6] "v"(oa[6]), [cr] "v"(crow), [qm] "s"(0xccccccccccccccccull),
        [g0] "v"(g0), [g1] "v"(g1), [mk] "v"(mk), [cw] "v"(cw), [so] "v"(so),
        [wm3] "s"(ws + WS_M3), [wsf] "s"(ws + WS_FRAG), [ql] "s"(0xfffffffffffull), [el] "s"(1ull << 47)
      : "v36", "v37", "v38", "v39", "v40", "v41", "v42", "v43", "v44", "v45", "v46", "v47", "v48", "v49", "v50", "v51", "v52", "v53", "v54", "v55", "v56", "v57", "v58", "v59", "v60", "v61", "v62", "v63", "v64", "v65", "v66", "v67", "v68", "v69", "v70", "v71", "v72", "v73", "v74", "v75", "v76", "v77", "v78", "v79", "v80", "v81", "v82", "v83", "v84", "v85", "v86", "v87", "v88", "v89", "v90", "v91", "v92", "v93", "v94", "v95", "v96", "v97", "v98", "v99", "v100", "v101", "v102", "v103", "v104", "v105", "v106", "v107", "v108", "v109", "v110", "v111", "v112", "v113", "v114", "v115", "v116", "v117", "v118", "v119", "v120", "v121", "v122", "v123", "v124", "v125", "v126", "v127", "v128", "v129", "v130", "v131", "v132", "v133", "v134", "v135", "v136", "v137", "v138", "v139", "v140", "v141", "v142", "v143", "v144", "v145", "v146", "v147", "v148", "v149", "v150", "v151", "v152", "v153", "v154", "v155", "v156", "v157", "v158", "v159", "v160", "v161", "v162", "v163", "v164", "v165", "v166", "v167", "v168", "v169", "v170", "v171", "v172", "v173", "v174", "v175", "v176", "v177", "v178", "v179", "v180", "v181", "v182", "v183", "v200", "v201", "v202", "v203", "v204", "v205", "v206", "v207", "v208", "v209", "v210", "v211", "v212", "v213", "v214", "v215", "v216", "v217", "v218", "v219", "v220", "v221", "v222", "v223", "v224", "v225", "v226", "v227", "v228", "v229", "v230", "v231", "v232", "v233", "v234", "v235", "v236", "v237", "v238", "v239", "v240", "v241", "v242", "v243", "v244", "v245", "v246", "v247", "v248", "v249", "v250", "v251", "s60", "s61", "s62", "s63", "s64", "s65", "s66", "s67", "s68", "s69", "s70", "s71", "s72", "s73", "s74", "s75", "vcc", "scc", "memory");
  else
    asm volatile(
      "v_mov_b32 v104, 100160\n"
      "v_add_u32_sdwa v36, v112, v104 dst_sel:DWORD dst_unused:UNUSED_PAD src0_sel:WORD_0 src1_sel:DWORD\n"
      "v_add_u32_sdwa v37, v112, v104 dst_sel:DWORD dst_unused:UNUSED_PAD src0_sel:WORD_1 src1_sel:DWORD\n"
      "v_add_u32_sdwa v38, v113, v104 dst_sel:DWORD dst_unused:UNUSED_PAD src0_sel:WORD_0 src1_sel:DWORD\n"
      "v_add_u32_sdwa v39, v113, v104 dst_sel:DWORD dst_unused:UNUSED_PAD src0_sel:WORD_1 src1_sel:DWORD\n"
      "v_add_u32_sdwa v40, v114, v104 dst_sel:DWORD dst_unused:UNUSED_PAD src0_sel:WORD_0 src1_sel:DWORD\n"
      "v_add_u32_sdwa v41, v114, v104 dst_sel:DWORD dst_unused:UNUSED_PAD src0_sel:WORD_1 src1_sel:DWORD\n"
      "v_add_u32_sdwa v42, v115, v104 dst_sel:DWORD dst_unused:UNUSED_PAD src0_sel:WORD_0 src1_sel:DWORD\n"
      "v_add_u32_sdwa v43, v115, v104 dst_sel:DWORD dst_unused:UNUSED_PAD src0_sel:WORD_1 src1_sel:DWORD\n"
      "v_add_u32_sdwa v44, v116, v104 dst_sel:DWORD dst_unused:UNUSED_PAD src0_sel:WORD_0 src1_sel:DWORD\n"
      "v_add_u32_sdwa v45, v116, v104 dst_sel:DWORD dst_unused:UNUSED_PAD src0_sel:WORD_1 src1_sel:DWORD\n"
      "v_add_u32_sdwa v46, v117, v104 dst_sel:DWORD dst_unused:UNUSED_PAD src0_sel:WORD_0 src1_sel:DWORD\n"
      "v_add_u32_sdwa v47, v117, v104 dst_sel:DWORD dst_unused:UNUSED_PAD src0_sel:WORD_1 src1_sel:DWORD\n"
      "v_add_u32_sdwa v48, v118, v104 dst_sel:DWORD dst_unused:UNUSED_PAD src0_sel:WORD_0 src1_sel:DWORD\n"
      "v_add_u32_sdwa v49, v118, v104 dst_sel:DWORD dst_unused:UNUSED_PAD src0_sel:WORD_1 src1_sel:DWORD\n"
      "v_add_u32_sdwa v50, v119, v104 dst_sel:DWORD dst_unused:UNUSED_PAD src0_sel:WORD_0 src1_sel:DWORD\n"
      "v_add_u32_sdwa v51, v119, v104 dst_sel:DWORD dst_unused:UNUSED_PAD src0_sel:WORD_1 src1_sel:DWORD\n"
      "v_readfirstlane_b32 s71, v250\n"
      "s_add_u32 s64, s71, 0\n"
      "s_bfe_u32 s68, s64, 0x20002\n"
      "s_and_b32 s64, s64, 3\n"
      "s_lshl_b32 s68, s68, 16\n"
      "s_or_b32 s64, s64, s68\n"
      "s_add_u32 s65, s71, 5\n"
      "s_bfe_u32 s68, s65, 0x20002\n"
      "s_and_b32 s65, s65, 3\n"
      "s_lshl_b32 s68, s68, 16\n"
      "s_or_b32 s65, s65, s68\n"
      "s_add_u32 s66, s71, 10\n"
      "s_bfe_u32 s68, s66, 0x20002\n"
      "s_and_b32 s66, s66, 3\n"
      "s_lshl_b32 s68, s68, 16\n"
      "s_or_b32 s66, s66, s68\n"
      "s_add_u32 s67, s71, 15\n"
      "s_bfe_u32 s68, s67, 0x20002\n"
      "s_and_b32 s67, s67, 3\n"
      "s_lshl_b32 s68, s68, 16\n"
      "s_or_b32 s67, s67, s68\n"
      "ds_read_b32 v84, v36\n"
      "ds_read_b32 v85, v37\n"
      "ds_read_b32 v86, v38\n"
      "ds_read_b32 v87, v39\n"
      "ds_read_b32 v88, v40\n"
      "ds_read_b32 v89, v41\n"
      "ds_read_b32 v90, v42\n"
      "ds_read_b32 v91, v43\n"
      "ds_read_b32 v92, v44\n"
      "ds_read_b32 v93, v45\n"
      "ds_read_b32 v94, v46\n"
      "ds_read_b32 v95, v47\n"
      "ds_read_b32 v96, v48\n"
      "ds_read_b32 v97, v49\n"
      "ds_read_b32 v98, v50\n"
      "ds_read_b32 v99, v51\n"
      "s_mov_b64 s[72:73], %[wsf]\n"
      "s_and_b32 s73, s73, 0xffff\n"
      "s_mov_b32 s74, 39936\n"
      "s_mov_b32 s75, 0x20000\n"
      "s_mov_b32 s69, 0x20002\n"
      "s_mov_b32 s70, 0xfffcfffc\n"
      "s_waitcnt lgkmcnt(8)\n"
      "v_add_f32 v104, v84, v85\n"
      "v_add_f32 v105, v86, v87\n"
      "v_add_f32 v106, v88, v89\n"
      "v_add_f32 v107, v90, v91\n"
      "s_waitcnt lgkmcnt(0)\n"
      "v_add_f32 v108, v92, v93\n"
      "v_add_f32 v109, v94, v95\n"
      "v_add_f32 v110, v96, v97\n"
      "v_add_f32 v111, v98, v99\n"
      "v_max3_f32 v84, |v104|, |v105|, |v106|\n"
      "v_max3_f32 v85, |v108|, |v109|, |v110|\n"
      "v_max3_f32 v84, |v107|, |v111|, v84\n"
      "v_mov_b32 v87, 0xfffff000\n"
      "v_max_f32 v84, v84, v85\n"
      "s_nop 0\n"
      "v_frexp_exp_i32_f32 v85, v84\n"
      "v_cmp_lt_f32 vcc, 0, v84\n"
      "s_nop 1\n"
      "v_cndmask_b32 v85, v87, v85, vcc\n"
      "s_nop 1\n"
      "v_max_i32_dpp v85, v85, v85 quad_perm:[1,0,3,2] row_mask:0xf bank_mask:0xf\n"
      "s_nop 1\n"
      "v_max_i32_dpp v85, v85, v85 quad_perm:[2,3,0,1] row_mask:0xf bank_mask:0xf\n"
      "s_nop 1\n"
      "v_max_i32_dpp v85, v85, v85 row_half_mirror row_mask:0xf bank_mask:0xf\n"
      "s_nop 1\n"
      "v_max_i32_dpp v85, v85, v85 row_mirror row_mask:0xf bank_mask:0xf\n"
      "s_nop 1\n"
      "v_max_i32_dpp v85, v85, v85 row_bcast:15 row_mask:0xa bank_mask:0xf\n"
      "s_nop 1\n"
      "v_max_i32_dpp v85, v85, v85 row_bcast:31 row_mask:0xc bank_mask:0xf\n"
      "s_nop 1\n"
      "v_readlane_b32 s71, v85, 63\n"
      "s_sub_u32 s71, s71, 8\n"
      "s_and_b32 s71, s71, -8\n"
      "s_sub_u32 s68, 0, s71\n"
      "s_and_b32 s71, s71, 0x3fff\n"
      "s_lshl_b32 s71, s71, 2\n"
      "v_ldexp_f32 v104, v104, s68\n"
      "v_ldexp_f32 v105, v105, s68\n"
      "v_ldexp_f32 v106, v106, s68\n"
      "v_ldexp_f32 v107, v107, s68\n"
      "v_ldexp_f32 v108, v108, s68\n"
      "v_ldexp_f32 v109, v109, s68\n"
      "v_ldexp_f32 v110, v110, s68\n"
      "v_ldexp_f32 v111, v111, s68\n"
      "v_cvt_pk_f16_f32 v88, v104, v105\n"
      "v_cvt_pk_f16_f32 v89, v106, v107\n"
      "v_cvt_pk_f16_f32 v90, v108, v109\n"
      "v_cvt_pk_f16_f32 v91, v110, v111\n"
      "v_add_u32 v88, s69, v88\n"
      "v_add_u32 v89, s69, v89\n"
      "v_add_u32 v90, s69, v90\n"
      "v_add_u32 v91, s69, v91\n"
      "s_mov_b64 exec, %[el]\n"
      "v_mov_b32 v88, s71\n"
      "s_mov_b64 exec, -1\n"
      "v_and_b32 v88, s70, v88\n"
      "v_and_b32 v89, s70, v89\n"
      "v_and_b32 v90, s70, v90\n"
      "v_and_b32 v91, s70, v91\n"
      "v_or_b32 v88, s64, v88\n"
      "v_or_b32 v89, s65, v89\n"
      "v_or_b32 v90, s66, v90\n"
      "v_or_b32 v91, s67, v91\n"
      "s_mov_b32 exec_hi, 0xffff\n"
      "buffer_store_dwordx4 v[88:91], %[so], s[72:75], 0 offen sc1\n"
      "s_mov_b32 exec_hi, -1\n"
      "s_nop 1\n"
      "ds_read_b128 v[200:203], %[tba] offset:0\n"
      "ds_read_b128 v[204:207], %[tba] offset:4096\n"
      "ds_read_b128 v[208:211], %[tba] offset:8192\n"
      "ds_read_b128 v[212:215], %[tba] offset:12288\n"
      "ds_read_b128 v[216:219], %[tba] offset:16384\n"
      "ds_read_b128 v[220:223], %[tba] offset:20480\n"
      "ds_read_b128 v[224:227], %[tba] offset:24576\n"
      "ds_read_b128 v[228:231], %[tb7]\n"
      "v_mov_b32 v182, 61376\n"
      "v_mov_b32 v183, 1\n"
      "s_mov_b32 s60, 0x7fff7fff\n"
      "s_mov_b32 s62, 0x10001\n"
      "s_mov_b32 s63, s62\n"
      "s_waitcnt lgkmcnt(6)\n"
      "v_pk_maximum3_f16 v132, v200, v204, v200\n"
      "v_pk_minimum3_f16 v136, v200, v204, v200\n"
      "v_pk_maximum3_f16 v133, v201, v205, v201\n"
      "v_pk_minimum3_f16 v137, v201, v205, v201\n"
      "v_pk_maximum3_f16 v134, v202, v206, v202\n"
      "v_pk_minimum3_f16 v138, v202, v206, v202\n"
      "v_pk_maximum3_f16 v135, v203, v207, v203\n"
      "v_pk_minimum3_f16 v139, v203, v207, v203\n"
      "s_waitcnt lgkmcnt(4)\n"
      "v_pk_maximum3_f16 v132, v208, v212, v132\n"
      "v_pk_minimum3_f16 v136, v208, v212, v136\n"
      "v_pk_maximum3_f16 v133, v209, v213, v133\n"
      "v_pk_minimum3_f16 v137, v209, v213, v137\n"
      "v_pk_maximum3_f16 v134, v210, v214, v134\n"
      "v_pk_minimum3_f16 v138, v210, v214, v138\n"
      "v_pk_maximum3_f16 v135, v211, v215, v135\n"
      "v_pk_minimum3_f16 v139, v211, v215, v139\n"
      "ds_read_u16 v164, %[oa0] offset:0\n"
      "ds_read_u16 v165, %[oa1] offset:0\n"
      "ds_read_u16 v166, %[oa2] offset:0\n"
      "ds_read_u16 v167, %[oa3] offset:0\n"
      "ds_read_u16 v168, %[oa4] offset:0\n"
      "ds_read_u16 v169, %[oa5] offset:0\n"
      "ds_read_u16 v170, %[oa6] offset:0\n"
      "ds_read_u16 v171, %[oa0] offset:416\n"
      "ds_read_u16 v172, %[oa1] offset:416\n"
      "ds_read_u16 v173, %[oa2] offset:416\n"
      "ds_read_u16 v174, %[oa3] offset:416\n"
      "ds_read_u16 v175, %[oa4] offset:416\n"
      "ds_read_u16 v176, %[oa5] offset:416\n"
      "ds_read_u16 v177, %[oa6] offset:416\n"
      "ds_read_b64 v[130:131], v182 offset:56\n"
      "s_waitcnt lgkmcnt(15)\n"
      "v_pk_maximum3_f16 v132, v216, v220, v132\n"
      "v_pk_minimum3_f16 v136, v216, v220, v136\n"
      "v_pk_maximum3_f16 v133, v217, v221, v133\n"
      "v_pk_minimum3_f16 v137, v217, v221, v137\n"
      "v_pk_maximum3_f16 v134, v218, v222, v134\n"
      "v_pk_minimum3_f16 v138, v218, v222, v138\n"
      "v_pk_maximum3_f16 v135, v219, v223, v135\n"
      "v_pk_minimum3_f16 v139, v219, v223, v139\n"
      "v_pk_maximum3_f16 v132, v224, v228, v132\n"
      "v_pk_minimum3_f16 v136, v224, v228, v136\n"
      "v_pk_maximum3_f16 v133, v225, v229, v133\n"
      "v_pk_minimum3_f16 v137, v225, v229, v137\n"
      "v_pk_maximum3_f16 v134, v226, v230, v134\n"
      "v_pk_minimum3_f16 v138, v226, v230, v138\n"
      "v_pk_maximum3_f16 v135, v227, v231, v135\n"
      "v_pk_minimum3_f16 v139, v227, v231, v139\n"
      "v_and_b32 v132, s60, v132\n"
      "v_and_b32 v133, s60, v133\n"
      "v_and_b32 v134, s60, v134\n"
      "v_and_b32 v135, s60, v135\n"
      "v_and_b32 v136, s60, v136\n"
      "v_and_b32 v137, s60, v137\n"
      "v_and_b32 v138, s60, v138\n"
      "v_and_b32 v139, s60, v139\n"
      "v_pk_max_u16 v178, v132, v136\n"
      "v_pk_max_u16 v179, v133, v137\n"
      "v_pk_max_u16 v180, v134, v138\n"
      "v_pk_max_u16 v181, v135, v139\n"
      "v_pk_max_u16 v178, v178, v179\n"
      "v_pk_max_u16 v180, v180, v181\n"
      "s_waitcnt lgkmcnt(0)\n"
      "v_pk_max_u16 v178, v178, v180\n"
      "v_cndmask_b32_e64 v167, v167, %[cr], %[qm]\n"
      "v_max_u16_sdwa v178, v178, v178 dst_sel:DWORD dst_unused:UNUSED_PAD src0_sel:WORD_0 src1_sel:WORD_1\n"
      "v_lshl_or_b32 v164, v165, 16, v164\n"
      "v_lshl_or_b32 v168, v169, 16, v168\n"
      "v_max_u32_dpp v178, v178, v178 quad_perm:[1,0,3,2] row_mask:0xf bank_mask:0xf\n"
      "v_lshl_or_b32 v165, v167, 16, v166\n"
      "v_cndmask_b32_e64 v174, v174, %[cr], %[qm]\n"
      "v_max_u32_dpp v178, v178, v178 quad_perm:[2,3,0,1] row_mask:0xf bank_mask:0xf\n"
      "v_lshl_or_b32 v171, v172, 16, v171\n"
      "v_lshl_or_b32 v175, v176, 16, v175\n"
      "v_max_u32_dpp v178, v178, v178 row_half_mirror row_mask:0xf bank_mask:0xf\n"
      "v_lshl_or_b32 v172, v174, 16, v173\n"
      "v_mov_b32 v166, v168\n"
      "v_max_u32_dpp v178, v178, v178 row_mirror row_mask:0xf bank_mask:0xf\n"
      "v_mov_b32 v167, v170\n"
      "v_mov_b32 v168, v171\n"
      "s_mov_b64 exec, s[62:63]\n"
      "ds_max_u32 v182, v178 offset:32\n"
      "s_mov_b64 exec, 1\n"
      "ds_add_u32 v182, v183 offset:36\n"
      "s_mov_b64 exec, -1\n"
      "v_mov_b32 v169, v172\n"
      "v_mov_b32 v170, v175\n"
      "v_mov_b32 v171, v177\n"
      "ds_write_b128 %[ow], v[164:167]\n"
      "ds_write_b128 %[ow], v[168:171] offset:1024\n"
      "v_readfirstlane_b32 %[epl], v130\n"
      "v_readfirstlane_b32 %[eph], v131\n"
      "s_branch 2f\n"
      "1:\n"
      "s_sleep 1\n"
      "2:\n"
      "ds_read_b64 v[178:179], v182 offset:32\n"
      "s_waitcnt lgkmcnt(0)\n"
      "v_cmp_gt_u32 vcc, 4, v179\n"
      "s_cbranch_vccnz 1b\n"
      "v_readfirstlane_b32 s60, v178\n"
      "s_lshr_b32 s61, s60, 7\n"
      "s_max_u32 s61, s61, 22\n"
      "s_sub_u32 s62, s61, 134\n"
      "s_cmp_lg_u32 s60, 0\n"
      "s_cselect_b32 %[ex], s62, 0\n"
      : [ex] "=&s"(ex), [epl] "=&s"(epl), [eph] "=&s"(eph)
      : [tba] "v"(tba), [tb7] "v"(tb7), [ow] "v"(ow), [oa0] "v"(oa[0]), [oa1] "v"(oa[1]), [oa2] "v"(oa[2]), [oa3] "v"(oa[3]),
        [oa4] "v"(oa[4]), [oa5] "v"(oa[5]), [oa6] "v"(oa[6]), [cr] "v"(crow), [qm] "s"(0xccccccccccccccccull),
        [so] "v"(so), [wsf] "s"(ws + WS_FRAG), [el] "s"(1ull << 44)
      : "v36", "v37", "v38", "v39", "v40", "v41", "v42", "v43", "v44", "v45", "v46", "v47", "v48", "v49", "v50", "v51", "v52", "v53", "v54", "v55", "v56", "v57", "v58", "v59", "v60", "v61", "v62", "v63", "v64", "v65", "v66", "v67", "v68", "v69", "v70", "v71", "v72", "v73", "v74", "v75", "v76", "v77", "v78", "v79", "v80", "v81", "v82", "v83", "v84", "v85", "v86", "v87", "v88", "v89", "v90", "v91", "v92", "v93", "v94", "v95", "v96", "v97", "v98", "v99", "v100", "v101", "v102", "v103", "v104", "v105", "v106", "v107", "v108", "v109", "v110", "v111", "v112", "v113", "v114", "v115", "v116", "v117", "v118", "v119", "v120", "v121", "v122", "v123", "v124", "v125", "v126", "v127", "v128", "v129", "v130", "v131", "v132", "v133", "v134", "v135", "v136", "v137", "v138", "v139", "v140", "v141", "v142", "v143", "v144", "v145", "v146", "v147", "v148", "v149", "v150", "v151", "v152", "v153", "v154", "v155", "v156", "v157", "v158", "v159", "v160", "v161", "v162", "v163", "v164", "v165", "v166", "v167", "v168", "v169", "v170", "v171", "v172", "v173", "v174", "v175", "v176", "v177", "v178", "v179", "v180", "v181", "v182", "v183", "v200", "v201", "v202", "v203", "v204", "v205", "v206", "v207", "v208", "v209", "v210", "v211", "v212", "v213", "v214", "v215", "v216", "v217", "v218", "v219", "v220", "v221", "v222", "v223", "v224", "v225", "v226", "v227", "v228", "v229", "v230", "v231", "v232", "v233", "v234", "v235", "v236", "v237", "v238", "v239", "v240", "v241", "v242", "v243", "v244", "v245", "v246", "v247", "v248", "v249", "v250", "v251", "s60", "s61", "s62", "s63", "s64", "s65", "s66", "s67", "s68", "s69", "s70", "s71", "s72", "s73", "s74", "s75", "vcc", "scc", "memory");
  ep = (u64)epl | ((u64)eph << 32);
  if (c == 2 && lane == 0) __hip_atomic_store((gu64*)(ws + WS_ACK) + blk * 16, ep, RLX);
  return ex;
}

__global__ __launch_bounds__(512, 2) void xdfm_main(const int* idx, const u16* table, const u16* wc, const u16* wco, const u16* w1,
                                                    const u16* b1, const u16* w2, const u16* bo, u16* out, char* ws) {
  __shared__ __attribute__((aligned(16))) char lds[LDS_TOTAL];
  asm volatile("" :: "s"((unsigned)(uintptr_t)(__attribute__((address_space(3))) char*)lds));
  const int wave = __builtin_amdgcn_readfirstlane(threadIdx.x >> 6), blk = blockIdx.x;
  {
    const u64 w3p = (u64)wc;
    const int rot = blk >> 3;
    asm volatile(
      "s_cmp_ge_u32 %[wv], 4\n"
      "s_cbranch_scc1 14f\n"
      "s_mov_b32 s58, m0\n"
      "s_setprio 2\n"
      "v_mbcnt_lo_u32_b32 v206, -1, 0\n"
      "v_mbcnt_hi_u32_b32 v206, -1, v206\n"
      "s_lshl_b32 s59, %[wv], 6\n"
      "v_add_u32 v21, s59, v206\n"
      "v_lshlrev_b32 v22, 4, v21\n"
      "global_load_dwordx4 v[0:3], v22, %[idx]\n"
      "v_min_u32 v23, 2544, v22\n"
      "v_add_u32 v23, 4096, v23\n"
      "global_load_dwordx4 v[4:7], v23, %[idx]\n"
      "v_min_u32 v24, 207, v21\n"
      "v_lshlrev_b32 v24, 2, v24\n"
      "global_load_dword v8, v24, %[b1]\n"
      "global_load_dword v9, v24, %[w2]\n"
      "s_mov_b64 s[48:49], %[tab]\n"
      "s_and_b32 s49, s49, 0xffff\n"
      "s_mov_b32 s50, 32000\n"
      "s_mov_b32 s51, 0x20000\n"
      "v_bfe_u32 v25, v206, 4, 1\n"
      "v_xor_b32 v25, v25, v206\n"
      "v_lshlrev_b32 v25, 4, v25\n"
      "s_lshl_b32 s59, %[wv], 13\n"
      "s_add_u32 s57, s59, 61504\n"
      "s_mov_b32 m0, s57\n"
      "s_cmp_eq_u32 %[wv], 3\n"
      "s_cselect_b64 s[52:53], 0xffff, -1\n"
      "buffer_load_dwordx4 v25, s[48:51], s59 offen offset:0 lds\n"
      "buffer_load_dwordx4 v25, s[48:51], s59 offen offset:1024 lds\n"
      "buffer_load_dwordx4 v25, s[48:51], s59 offen offset:2048 lds\n"
      "buffer_load_dwordx4 v25, s[48:51], s59 offen offset:3072 lds\n"
      "s_add_u32 s57, s57, 4096\n"
      "s_add_u32 s59, s59, 4096\n"
      "s_mov_b32 m0, s57\n"
      "s_nop 0\n"
      "buffer_load_dwordx4 v25, s[48:51], s59 offen offset:0 lds\n"
      "buffer_load_dwordx4 v25, s[48:51], s59 offen offset:1024 lds\n"
      "buffer_load_dwordx4 v25, s[48:51], s59 offen offset:2048 lds\n"
      "s_mov_b64 exec, s[52:53]\n"
      "buffer_load_dwordx4 v25, s[48:51], s59 offen offset:3072 lds\n"
      "s_mov_b64 exec, -1\n"
      "s_mov_b64 s[44:45], %[w1]\n"
      "s_and_b32 s45, s45, 0xffff\n"
      "s_mov_b32 s46, 346112\n"
      "s_mov_b32 s47, 0x20000\n"
      "s_mul_i32 s59, %[wv], 192\n"
      "v_add_u32 v203, 0, v206\n"
      "v_add_u32 v204, 64, v206\n"
      "v_add_u32 v205, 128, v206\n"
      "v_mul_u32_u24 v203, 5462, v203\n"
      "v_mul_u32_u24 v204, 5462, v204\n"
      "v_mul_u32_u24 v205, 5462, v205\n"
      "v_lshl_add_u32 v26, v206, 4, s59\n"
      "v_lshrrev_b32 v203, 16, v203\n"
      "v_lshrrev_b32 v204, 16, v204\n"
      "v_lshrrev_b32 v205, 16, v205\n"
      "s_movk_i32 s57, 640\n"
      "v_mad_u32_u24 v203, v203, s57, v26\n"
      "v_mad_u32_u24 v204, v204, s57, v26\n"
      "v_mad_u32_u24 v205, v205, s57, v26\n"
      "s_mul_i32 s59, %[wv], 14336\n"
      "s_add_u32 s59, s59, 106304\n"
      "s_add_u32 s72, s59, 0\n"
      "s_add_u32 s73, s59, 3072\n"
      "s_add_u32 s74, s59, 6144\n"
      "s_add_u32 s75, s59, 9216\n"
      "s_add_u32 s78, s59, 12288\n"
      "s_add_u32 s79, s78, 1024\n"
      "s_mov_b32 m0, s72\n"
      "s_mov_b32 s40, 0\n"
      "buffer_load_dwordx4 v203, s[44:47], s40 offen offset:0 lds\n"
      "buffer_load_dwordx4 v204, s[44:47], s40 offen offset:1024 lds\n"
      "buffer_load_dwordx4 v205, s[44:47], s40 offen offset:2048 lds\n"
      "v_lshrrev_b32 v25, 2, v206\n"
      "v_mul_u32_u24 v25, 768, v25\n"
      "v_lshl_add_u32 v202, v206, 4, v25\n"
      "v_add_u32 v202, 768, v202\n"
      "s_mov_b32 m0, s78\n"
      "s_mul_i32 s41, %[wv], 13312\n"
      "buffer_load_dwordx4 v202, s[44:47], s41 offen lds\n"
      "s_branch 4f\n"
      "14:\n"
      "v_mbcnt_lo_u32_b32 v121, -1, 0\n"
      "v_mov_b32 v122, 0\n"
      "v_mbcnt_hi_u32_b32 v121, -1, v121\n"
      "s_cmp_eq_u32 %[wv], 4\n"
      "s_cbranch_scc1 3f\n"
      "s_mov_b32 s58, m0\n"
      "s_lshl_b32 s57, %[wv], 11\n"
      "s_add_u32 m0, s57, 44992\n"
      "s_cmp_eq_u32 %[wv], 7\n"
      "s_cbranch_scc1 5f\n"
      "s_lshl_b32 s57, %[wv], 6\n"
      "s_sub_u32 s57, s57, 320\n"
      "v_add_u32 v121, s57, v121\n"
      "v_min_u32 v121, 84, v121\n"
      "s_mul_i32 s57, %[rot], 85\n"
      "v_add_u32 v121, s57, v121\n"
      "v_min_u32 v121, 2703, v121\n"
      "v_lshlrev_b32 v121, 7, v121\n"
      "global_load_lds_dword v121, %[w1]\n"
      "s_mov_b32 m0, s58\n"
      "s_branch 9f\n"
      "5:\n"
      "v_and_b32 v121, 7, v121\n"
      "s_lshl_b32 s57, %[rot], 3\n"
      "v_add_u32 v121, s57, v121\n"
      "v_min_u32 v121, 200, v121\n"
      "v_lshlrev_b32 v121, 7, v121\n"
      "s_getpc_b64 s[54:55]\n"
      "s_and_b32 s54, s54, 0xffffff00\n"
      "s_nop 0\n"
      "global_load_lds_dword v121, s[54:55]\n"
      "s_mov_b32 m0, s58\n"
      "s_branch 9f\n"
      "3:\n"
      "global_load_dwordx2 v[250:251], v122, %[wep] sc1\n"
      "s_cmp_ge_u32 %[blk], 78\n"
      "s_cbranch_scc1 9f\n"
      "v_mov_b32 v240, 0\n"
      "v_mov_b32 v241, 0\n"
      "v_mov_b32 v242, 0\n"
      "v_mov_b32 v243, 0\n"
      "v_mov_b32 v244, 0\n"
      "v_mov_b32 v245, 0\n"
      "v_mov_b32 v246, 0\n"
      "v_mov_b32 v247, 0\n"
      "s_cmp_ge_u32 %[blk], 26\n"
      "s_cbranch_scc1 1f\n"
      "v_lshrrev_b32 v123, 4, v121\n"
      "v_and_b32 v124, 15, v121\n"
      "v_min_u32 v125, 12, v124\n"
      "v_lshlrev_b32 v125, 2, v125\n"
      "s_mul_i32 s57, %[blk], 52\n"
      "s_add_u32 s57, s57, 70304\n"
      "s_add_u32 s54, %[wclo], s57\n"
      "s_addc_u32 s55, %[wchi], 0\n"
      "v_mul_u32_u24 v126, 1352, v123\n"
      "v_min_u32 v127, 1352, v126\n"
      "v_add_u32 v126, v126, v125\n"
      "v_add_u32 v127, v127, v125\n"
      "v_add_u32 v127, 32448, v127\n"
      "global_load_dword v232, v126, s[54:55]\n"
      "v_add_u32 v126, 5408, v126\n"
      "global_load_dword v233, v126, s[54:55]\n"
      "v_add_u32 v126, 5408, v126\n"
      "global_load_dword v234, v126, s[54:55]\n"
      "v_add_u32 v126, 5408, v126\n"
      "global_load_dword v235, v126, s[54:55]\n"
      "v_add_u32 v126, 5408, v126\n"
      "global_load_dword v236, v126, s[54:55]\n"
      "v_add_u32 v126, 5408, v126\n"
      "global_load_dword v237, v126, s[54:55]\n"
      "global_load_dword v238, v127, s[54:55]\n"
      "v_lshlrev_b32 v123, 1, v123\n"
      "global_load_short_d16_hi v240, v123, %[wco] offset:104\n"
      "global_load_short_d16_hi v241, v123, %[wco] offset:112\n"
      "global_load_short_d16_hi v242, v123, %[wco] offset:120\n"
      "global_load_short_d16_hi v243, v123, %[wco] offset:128\n"
      "global_load_short_d16_hi v244, v123, %[wco] offset:136\n"
      "global_load_short_d16_hi v245, v123, %[wco] offset:144\n"
      "s_mov_b32 exec_hi, 0\n"
      "global_load_short_d16_hi v246, v123, %[wco] offset:152\n"
      "s_mov_b32 exec_hi, -1\n"
      "s_branch 9f\n"
      "1:\n"
      "v_min_u32 v123, 47, v121\n"
      "v_lshlrev_b32 v123, 5, v123\n"
      "global_load_dwordx4 v[112:115], v123, %[slt]\n"
      "global_load_dwordx4 v[116:119], v123, %[slt] offset:16\n"
      "s_cmp_ge_u32 %[blk], 52\n"
      "s_cbranch_scc1 2f\n"
      "v_mov_b32 v104, 0\n"
      "v_mov_b32 v105, 0\n"
      "v_mov_b32 v106, 0\n"
      "v_mov_b32 v107, 0\n"
      "v_mov_b32 v108, 0\n"
      "v_mov_b32 v109, 0\n"
      "v_mov_b32 v110, 0\n"
      "v_mov_b32 v111, 0\n"
      "s_mul_i32 s57, %[blk], 52\n"
      "s_add_u32 s57, s57, 33800\n"
      "s_add_u32 s54, %[wclo], s57\n"
      "s_addc_u32 s55, %[wchi], 0\n"
      "v_and_b32 v124, 31, v121\n"
      "v_lshrrev_b32 v125, 5, v121\n"
      "v_min_u32 v124, 25, v124\n"
      "v_lshlrev_b32 v124, 1, v124\n"
      "v_mul_u32_u24 v126, 10816, v125\n"
      "v_add_u32 v127, 33800, v124\n"
      "v_add_u32 v126, v126, v124\n"
      "global_load_ushort v232, v126, s[54:55]\n"
      "v_add_u32 v126, 1352, v126\n"
      "global_load_short_d16_hi v104, v126, s[54:55]\n"
      "v_add_u32 v126, 1352, v126\n"
      "global_load_ushort v233, v126, s[54:55]\n"
      "v_add_u32 v126, 1352, v126\n"
      "global_load_short_d16_hi v105, v126, s[54:55]\n"
      "v_add_u32 v126, 1352, v126\n"
      "global_load_ushort v234, v126, s[54:55]\n"
      "v_add_u32 v126, 1352, v126\n"
      "global_load_short_d16_hi v106, v126, s[54:55]\n"
      "v_add_u32 v126, 1352, v126\n"
      "global_load_ushort v235, v126, s[54:55]\n"
      "v_add_u32 v126, 1352, v126\n"
      "global_load_short_d16_hi v107, v126, s[54:55]\n"
      "v_add_u32 v126, 12168, v126\n"
      "global_load_ushort v236, v126, s[54:55]\n"
      "v_add_u32 v126, 1352, v126\n"
      "global_load_short_d16_hi v108, v126, s[54:55]\n"
      "v_add_u32 v126, 1352, v126\n"
      "v_min_u32 v126, v126, v127\n"
      "global_load_ushort v237, v126, s[54:55]\n"
      "v_add_u32 v126, 1352, v126\n"
      "v_min_u32 v126, v126, v127\n"
      "global_load_short_d16_hi v109, v126, s[54:55]\n"
      "v_add_u32 v126, 1352, v126\n"
      "v_min_u32 v126, v126, v127\n"
      "global_load_ushort v238, v126, s[54:55]\n"
      "v_add_u32 v126, 1352, v126\n"
      "v_min_u32 v126, v126, v127\n"
      "global_load_short_d16_hi v110, v126, s[54:55]\n"
      "v_add_u32 v126, 1352, v126\n"
      "v_min_u32 v126, v126, v127\n"
      "global_load_ushort v239, v126, s[54:55]\n"
      "v_add_u32 v126, 1352, v126\n"
      "v_min_u32 v126, v126, v127\n"
      "global_load_short_d16_hi v111, v126, s[54:55]\n"
      "v_lshlrev_b32 v125, 4, v125\n"
      "s_mov_b32 exec_lo, 0x4000000\n"
      "s_mov_b32 exec_hi, 0x4000000\n"
      "global_load_dword v240, v125, %[wco] offset:52\n"
      "global_load_dword v241, v125, %[wco] offset:56\n"
      "global_load_dword v242, v125, %[wco] offset:60\n"
      "global_load_dword v243, v125, %[wco] offset:64\n"
      "global_load_dword v244, v125, %[wco] offset:84\n"
      "s_mov_b32 exec_hi, 0\n"
      "global_load_dword v245, v125, %[wco] offset:88\n"
      "global_load_dword v246, v125, %[wco] offset:92\n"
      "global_load_dword v247, v125, %[wco] offset:96\n"
      "s_mov_b64 exec, -1\n"
      "s_lshl_b32 s57, %[blk], 1\n"
      "s_sub_u32 s57, s57, 52\n"
      "v_mov_b32 v124, s57\n"
      "v_mov_b32 v120, 0\n"
      "global_load_short_d16_hi v120, v124, %[wco]\n"
      "s_branch 9f\n"
      "2:\n"
      "s_mul_i32 s57, %[blk], 1352\n"
      "s_sub_u32 s57, s57, 70304\n"
      "s_add_u32 s54, %[wclo], s57\n"
      "s_addc_u32 s55, %[wchi], 0\n"
      "v_lshlrev_b32 v124, 2, v121\n"
      "v_min_u32 v125, 68, v124\n"
      "global_load_dword v232, v124, s[54:55] offset:0\n"
      "global_load_dword v233, v124, s[54:55] offset:256\n"
      "global_load_dword v234, v124, s[54:55] offset:512\n"
      "global_load_dword v235, v124, s[54:55] offset:768\n"
      "global_load_dword v236, v124, s[54:55] offset:1024\n"
      "global_load_dword v237, v125, s[54:55] offset:1280\n"
      "s_branch 9f\n"
      "4:\n"
      "v_lshrrev_b32 v200, 5, v206\n"
      "v_lshrrev_b32 v25, 2, v206\n"
      "v_and_b32 v25, 3, v25\n"
      "v_lshl_add_u32 v25, v200, 3, v25\n"
      "v_and_b32 v26, 16, v206\n"
      "v_lshlrev_b32 v26, 1, v26\n"
      "v_and_b32 v27, 3, v206\n"
      "v_lshl_add_u32 v26, v27, 3, v26\n"
      "v_mul_u32_u24 v185, 192, v25\n"
      "v_add3_u32 v185, v185, v26, s59\n"
      "v_lshlrev_b32 v199, 6, v25\n"
      "v_add3_u32 v199, v199, v26, s78\n"
      "v_lshlrev_b32 v200, 4, v200\n"
      "s_mul_i32 s57, %[wv], 384\n"
      "s_add_u32 s57, s57, 96832\n"
      "v_add_u32 v201, s57, v200\n"
      "v_and_b32 v25, 31, v206\n"
      "v_mul_u32_u24 v25, 52, v25\n"
      "v_add_u32 v207, 93504, v25\n"
      "v_lshrrev_b32 v28, 1, v22\n"
      "v_lshrrev_b32 v29, 1, v23\n"
      "v_lshlrev_b32 v30, 1, v24\n"
      "v_add_u32 v28, 93504, v28\n"
      "v_add_u32 v29, 93504, v29\n"
      "v_add_u32 v30, 96832, v30\n"
      "v_mov_b32 v26, 0\n"
      "v_lshlrev_b32 v27, 2, v206\n"
      "s_mov_b64 exec, 0x7ff\n"
      "ds_write_b32 v27, v26 offset:61376\n"
      "s_mov_b64 exec, -1\n"
      "s_waitcnt vmcnt(12)\n"
      "s_mov_b32 m0, s73\n"
      "s_mov_b32 s40, 13312\n"
      "buffer_load_dwordx4 v203, s[44:47], s40 offen offset:0 lds\n"
      "buffer_load_dwordx4 v204, s[44:47], s40 offen offset:1024 lds\n"
      "buffer_load_dwordx4 v205, s[44:47], s40 offen offset:2048 lds\n"
      "v_lshl_or_b32 v0, v1, 16, v0\n"
      "v_lshl_or_b32 v1, v3, 16, v2\n"
      "v_lshl_or_b32 v2, v5, 16, v4\n"
      "v_lshl_or_b32 v3, v7, 16, v6\n"
      "v_and_b32 v10, 0x80008, v0\n"
      "v_and_b32 v11, 0x80008, v1\n"
      "v_and_b32 v12, 0x80008, v2\n"
      "v_and_b32 v13, 0x80008, v3\n"
      "v_lshlrev_b32 v0, 5, v0\n"
      "v_lshlrev_b32 v1, 5, v1\n"
      "v_lshlrev_b32 v2, 5, v2\n"
      "v_lshlrev_b32 v3, 5, v3\n"
      "v_lshl_or_b32 v0, v10, 1, v0\n"
      "v_lshl_or_b32 v1, v11, 1, v1\n"
      "v_lshl_or_b32 v2, v12, 1, v2\n"
      "v_lshl_or_b32 v3, v13, 1, v3\n"
      "ds_write_b64 v28, v[0:1]\n"
      "ds_write_b64 v29, v[2:3]\n"
      "v_lshlrev_b32 v10, 16, v8\n"
      "v_and_b32 v11, 0xffff0000, v8\n"
      "v_lshlrev_b32 v12, 16, v9\n"
      "v_and_b32 v13, 0xffff0000, v9\n"
      "s_mov_b32 s57, 0x0d800000\n"
      "s_brev_b32 s54, -2\n"
      "v_max_f32_e64 v14, |v12|, s57\n"
      "v_max_f32_e64 v15, |v13|, s57\n"
      "v_rcp_f32 v14, v14\n"
      "v_rcp_f32 v15, v15\n"
      "s_nop 0\n"
      "v_bfi_b32 v12, s54, v14, v12\n"
      "v_bfi_b32 v13, s54, v15, v13\n"
      "ds_write_b64 v30, v[10:11]\n"
      "ds_write_b64 v30, v[12:13] offset:1664\n"
      "s_waitcnt vmcnt(3)\n"
      "ds_read_b64_tr_b16 v[128:129], v185 offset:0\n"
      "ds_read_b64_tr_b16 v[130:131], v185 offset:768\n"
      "ds_read_b64_tr_b16 v[132:133], v185 offset:64\n"
      "ds_read_b64_tr_b16 v[134:135], v185 offset:832\n"
      "ds_read_b64_tr_b16 v[136:137], v185 offset:128\n"
      "ds_read_b64_tr_b16 v[138:139], v185 offset:896\n"
      "ds_read_b64_tr_b16 v[152:153], v199 offset:0\n"
      "ds_read_b64_tr_b16 v[154:155], v199 offset:256\n"
      "s_mov_b32 m0, s79\n"
      "s_add_u32 s41, s41, 53248\n"
      "buffer_load_dwordx4 v202, s[44:47], s41 offen lds\n"
      "v_add_u32 v206, 1664, v207\n"
      "s_waitcnt lgkmcnt(0)\n"
      "s_barrier\n"
      "ds_read2_b32 v[172:173], v207 offset0:0 offset1:1\n"
      "ds_read2_b32 v[186:187], v206 offset0:0 offset1:1\n"
      "ds_read_b128 v[208:211], v201 offset:0\n"
      "ds_read_b128 v[212:215], v201 offset:32\n"
      "ds_read_b128 v[216:219], v201 offset:64\n"
      "ds_read_b128 v[220:223], v201 offset:96\n"
      "s_waitcnt lgkmcnt(4)\n"
      "v_xor_b32_sdwa v156, v172, v200 dst_sel:DWORD dst_unused:UNUSED_PAD src0_sel:WORD_0 src1_sel:DWORD\n"
      "v_xor_b32_sdwa v160, v186, v200 dst_sel:DWORD dst_unused:UNUSED_PAD src0_sel:WORD_0 src1_sel:DWORD\n"
      "ds_read_b128 v[156:159], v156 offset:61504\n"
      "ds_read_b128 v[160:163], v160 offset:61504\n"
      "ds_read_b128 v[224:227], v201 offset:128\n"
      "ds_read_b128 v[228:231], v201 offset:160\n"
      "ds_read_b128 v[232:235], v201 offset:192\n"
      "ds_read_b128 v[236:239], v201 offset:224\n"
      "ds_read_b128 v[240:243], v201 offset:256\n"
      "ds_read_b128 v[244:247], v201 offset:288\n"
      "ds_read_b128 v[248:251], v201 offset:320\n"
      "ds_read_b128 v[252:255], v201 offset:352\n"
      "s_mov_b32 m0, s74\n"
      "s_mov_b32 s40, 26624\n"
      "buffer_load_dwordx4 v203, s[44:47], s40 offen offset:0 lds\n"
      "buffer_load_dwordx4 v204, s[44:47], s40 offen offset:1024 lds\n"
      "buffer_load_dwordx4 v205, s[44:47], s40 offen offset:2048 lds\n"
      "s_mov_b32 m0, s75\n"
      "s_mov_b32 s40, 39936\n"
      "buffer_load_dwordx4 v203, s[44:47], s40 offen offset:0 lds\n"
      "buffer_load_dwordx4 v204, s[44:47], s40 offen offset:1024 lds\n"
      "buffer_load_dwordx4 v205, s[44:47], s40 offen offset:2048 lds\n"
      "s_waitcnt vmcnt(6) lgkmcnt(0)\n"
      "v_mfma_f32_32x32x16_bf16 v[0:15], v[128:131], v[156:159], v[208:223]\n"
      "ds_read2_b32 v[174:175], v207 offset0:2 offset1:3\n"
      "ds_read2_b32 v[188:189], v206 offset0:2 offset1:3\n"
      "v_xor_b32_sdwa v164, v172, v200 dst_sel:DWORD dst_unused:UNUSED_PAD src0_sel:WORD_1 src1_sel:DWORD\n"
      "ds_read_b128 v[164:167], v164 offset:61504\n"
      "v_xor_b32_sdwa v168, v186, v200 dst_sel:DWORD dst_unused:UNUSED_PAD src0_sel:WORD_1 src1_sel:DWORD\n"
      "v_mfma_f32_32x32x16_bf16 v[16:31], v[128:131], v[160:163], v[208:223]\n"
      "ds_read_b128 v[168:171], v168 offset:61504\n"
      "ds_read_b64_tr_b16 v[140:141], v185 offset:3072\n"
      "ds_read_b64_tr_b16 v[142:143], v185 offset:3840\n"
      "s_mov_b32 m0, s72\n"
      "v_mfma_f32_32x32x16_bf16 v[32:47], v[132:135], v[156:159], v[224:239]\n"
      "ds_read_b64_tr_b16 v[144:145], v185 offset:3136\n"
      "ds_read_b64_tr_b16 v[146:147], v185 offset:3904\n"
      "ds_read_b64_tr_b16 v[148:149], v185 offset:3200\n"
      "ds_read_b64_tr_b16 v[150:151], v185 offset:3968\n"
      "v_mfma_f32_32x32x16_bf16 v[48:63], v[132:135], v[160:163], v[224:239]\n"
      "s_mov_b32 s40, 53248\n"
      "buffer_load_dwordx4 v203, s[44:47], s40 offen offset:0 lds\n"
      "buffer_load_dwordx4 v204, s[44:47], s40 offen offset:1024 lds\n"
      "v_mfma_f32_32x32x16_bf16 v[64:79], v[136:139], v[156:159], v[240:255]\n"
      "buffer_load_dwordx4 v205, s[44:47], s40 offen offset:2048 lds\n"
      "v_mfma_f32_32x32x16_bf16 v[80:95], v[136:139], v[160:163], v[240:255]\n"
      "s_cmp_lg_u32 %[wv], 0\n"
      "s_cbranch_scc1 102f\n"
      "ds_read_b64_tr_b16 v[208:209], v199 offset:1024\n"
      "ds_read_b64_tr_b16 v[210:211], v199 offset:1280\n"
      "v_mfma_f32_32x32x16_bf16 v[96:111], v[152:155], v[156:159], 0\n"
      "s_mov_b32 m0, s78\n"
      "s_mov_b32 s41, 106496\n"
      "buffer_load_dwordx4 v202, s[44:47], s41 offen lds\n"
      "v_mfma_f32_32x32x16_bf16 v[112:127], v[152:155], v[160:163], 0\n"
      "102:\n"
      "s_waitcnt vmcnt(6) lgkmcnt(0)\n"
      "v_mfma_f32_32x32x16_bf16 v[0:15], v[140:143], v[164:167], v[0:15]\n"
      "ds_read2_b32 v[176:177], v207 offset0:4 offset1:5\n"
      "ds_read2_b32 v[190:191], v206 offset0:4 offset1:5\n"
      "v_xor_b32_sdwa v156, v173, v200 dst_sel:DWORD dst_unused:UNUSED_PAD src0_sel:WORD_0 src1_sel:DWORD\n"
      "ds_read_b128 v[156:159], v156 offset:61504\n"
      "v_xor_b32_sdwa v160, v187, v200 dst_sel:DWORD dst_unused:UNUSED_PAD src0_sel:WORD_0 src1_sel:DWORD\n"
      "v_mfma_f32_32x32x16_bf16 v[16:31], v[140:143], v[168:171], v[16:31]\n"
      "ds_read_b128 v[160:163], v160 offset:61504\n"
      "ds_read_b64_tr_b16 v[128:129], v185 offset:6144\n"
      "ds_read_b64_tr_b16 v[130:131], v185 offset:6912\n"
      "s_mov_b32 m0, s73\n"
      "v_mfma_f32_32x32x16_bf16 v[32:47], v[144:147], v[164:167], v[32:47]\n"
      "ds_read_b64_tr_b16 v[132:133], v185 offset:6208\n"
      "ds_read_b64_tr_b16 v[134:135], v185 offset:6976\n"
      "ds_read_b64_tr_b16 v[136:137], v185 offset:6272\n"
      "ds_read_b64_tr_b16 v[138:139], v185 offset:7040\n"
      "v_mfma_f32_32x32x16_bf16 v[48:63], v[144:147], v[168:171], v[48:63]\n"
      "s_mov_b32 s40, 66560\n"
      "buffer_load_dwordx4 v203, s[44:47], s40 offen offset:0 lds\n"
      "buffer_load_dwordx4 v204, s[44:47], s40 offen offset:1024 lds\n"
      "v_mfma_f32_32x32x16_bf16 v[64:79], v[148:151], v[164:167], v[64:79]\n"
      "buffer_load_dwordx4 v205, s[44:47], s40 offen offset:2048 lds\n"
      "v_mfma_f32_32x32x16_bf16 v[80:95], v[148:151], v[168:171], v[80:95]\n"
      "s_cmp_lg_u32 %[wv], 1\n"
      "s_cbranch_scc1 112f\n"
      "ds_read_b64_tr_b16 v[208:209], v199 offset:1024\n"
      "ds_read_b64_tr_b16 v[210:211], v199 offset:1280\n"
      "v_mfma_f32_32x32x16_bf16 v[96:111], v[152:155], v[164:167], 0\n"
      "s_mov_b32 m0, s78\n"
      "s_mov_b32 s41, 119808\n"
      "buffer_load_dwordx4 v202, s[44:47], s41 offen lds\n"
      "v_mfma_f32_32x32x16_bf16 v[112:127], v[152:155], v[168:171], 0\n"
      "112:\n"
      "s_waitcnt vmcnt(6) lgkmcnt(0)\n"
      "v_mfma_f32_32x32x16_bf16 v[0:15], v[128:131], v[156:159], v[0:15]\n"
      "ds_read2_b32 v[178:179], v207 offset0:6 offset1:7\n"
      "ds_read2_b32 v[192:193], v206 offset0:6 offset1:7\n"
      "v_xor_b32_sdwa v164, v173, v200 dst_sel:DWORD dst_unused:UNUSED_PAD src0_sel:WORD_1 src1_sel:DWORD\n"
      "ds_read_b128 v[164:167], v164 offset:61504\n"
      "v_xor_b32_sdwa v168, v187, v200 dst_sel:DWORD dst_unused:UNUSED_PAD src0_sel:WORD_1 src1_sel:DWORD\n"
      "v_mfma_f32_32x32x16_bf16 v[16:31], v[128:131], v[160:163], v[16:31]\n"
      "ds_read_b128 v[168:171], v168 offset:61504\n"
      "ds_read_b64_tr_b16 v[140:141], v185 offset:9216\n"
      "ds_read_b64_tr_b16 v[142:143], v185 offset:9984\n"
      "s_mov_b32 m0, s74\n"
      "v_mfma_f32_32x32x16_bf16 v[32:47], v[132:135], v[156:159], v[32:47]\n"
      "ds_read_b64_tr_b16 v[144:145], v185 offset:9280\n"
      "ds_read_b64_tr_b16 v[146:147], v185 offset:10048\n"
      "ds_read_b64_tr_b16 v[148:149], v185 offset:9344\n"
      "ds_read_b64_tr_b16 v[150:151], v185 offset:10112\n"
      "v_mfma_f32_32x32x16_bf16 v[48:63], v[132:135], v[160:163], v[48:63]\n"
      "s_mov_b32 s40, 79872\n"
      "buffer_load_dwordx4 v203, s[44:47], s40 offen offset:0 lds\n"
      "buffer_load_dwordx4 v204, s[44:47], s40 offen offset:1024 lds\n"
      "v_mfma_f32_32x32x16_bf16 v[64:79], v[136:139], v[156:159], v[64:79]\n"
      "buffer_load_dwordx4 v205, s[44:47], s40 offen offset:2048 lds\n"
      "v_mfma_f32_32x32x16_bf16 v[80:95], v[136:139], v[160:163], v[80:95]\n"
      "s_cmp_lg_u32 %[wv], 2\n"
      "s_cbranch_scc1 122f\n"
      "ds_read_b64_tr_b16 v[208:209], v199 offset:1024\n"
      "ds_read_b64_tr_b16 v[210:211], v199 offset:1280\n"
      "v_mfma_f32_32x32x16_bf16 v[96:111], v[152:155], v[156:159], 0\n"
      "s_mov_b32 m0, s78\n"
      "s_mov_b32 s41, 133120\n"
      "buffer_load_dwordx4 v202, s[44:47], s41 offen lds\n"
      "v_mfma_f32_32x32x16_bf16 v[112:127], v[152:155], v[160:163], 0\n"
      "122:\n"
      "s_waitcnt vmcnt(6) lgkmcnt(0)\n"
      "v_mfma_f32_32x32x16_bf16 v[0:15], v[140:143], v[164:167], v[0:15]\n"
      "ds_read2_b32 v[180:181], v207 offset0:8 offset1:9\n"
      "ds_read2_b32 v[194:195], v206 offset0:8 offset1:9\n"
      "v_xor_b32_sdwa v156, v174, v200 dst_sel:DWORD dst_unused:UNUSED_PAD src0_sel:WORD_0 src1_sel:DWORD\n"
      "ds_read_b128 v[156:159], v156 offset:61504\n"
      "v_xor_b32_sdwa v160, v188, v200 dst_sel:DWORD dst_unused:UNUSED_PAD src0_sel:WORD_0 src1_sel:DWORD\n"
      "v_mfma_f32_32x32x16_bf16 v[16:31], v[140:143], v[168:171], v[16:31]\n"
      "ds_read_b128 v[160:163], v160 offset:61504\n"
      "ds_read_b64_tr_b16 v[128:129], v185 offset:0\n"
      "ds_read_b64_tr_b16 v[130:131], v185 offset:768\n"
      "s_mov_b32 m0, s75\n"
      "v_mfma_f32_32x32x16_bf16 v[32:47], v[144:147], v[164:167], v[32:47]\n"
      "ds_read_b64_tr_b16 v[132:133], v185 offset:64\n"
      "ds_read_b64_tr_b16 v[134:135], v185 offset:832\n"
      "ds_read_b64_tr_b16 v[136:137], v185 offset:128\n"
      "ds_read_b64_tr_b16 v[138:139], v185 offset:896\n"
      "v_mfma_f32_32x32x16_bf16 v[48:63], v[144:147], v[168:171], v[48:63]\n"
      "s_mov_b32 s40, 93184\n"
      "buffer_load_dwordx4 v203, s[44:47], s40 offen offset:0 lds\n"
      "buffer_load_dwordx4 v204, s[44:47], s40 offen offset:1024 lds\n"
      "v_mfma_f32_32x32x16_bf16 v[64:79], v[148:151], v[164:167], v[64:79]\n"
      "buffer_load_dwordx4 v205, s[44:47], s40 offen offset:2048 lds\n"
      "v_mfma_f32_32x32x16_bf16 v[80:95], v[148:151], v[168:171], v[80:95]\n"
      "s_cmp_lg_u32 %[wv], 3\n"
      "s_cbranch_scc1 132f\n"
      "ds_read_b64_tr_b16 v[208:209], v199 offset:1024\n"
      "ds_read_b64_tr_b16 v[210:211], v199 offset:1280\n"
      "v_mfma_f32_32x32x16_bf16 v[96:111], v[152:155], v[164:167], 0\n"
      "s_mov_b32 m0, s78\n"
      "s_mov_b32 s41, 146432\n"
      "buffer_load_dwordx4 v202, s[44:47], s41 offen lds\n"
      "v_mfma_f32_32x32x16_bf16 v[112:127], v[152:155], v[168:171], 0\n"
      "132:\n"
      "s_waitcnt vmcnt(6) lgkmcnt(0)\n"
      "v_mfma_f32_32x32x16_bf16 v[0:15], v[128:131], v[156:159], v[0:15]\n"
      "ds_read2_b32 v[182:183], v207 offset0:10 offset1:11\n"
      "ds_read2_b32 v[196:197], v206 offset0:10 offset1:11\n"
      "v_xor_b32_sdwa v164, v174, v200 dst_sel:DWORD dst_unused:UNUSED_PAD src0_sel:WORD_1 src1_sel:DWORD\n"
      "ds_read_b128 v[164:167], v164 offset:61504\n"
      "v_xor_b32_sdwa v168, v188, v200 dst_sel:DWORD dst_unused:UNUSED_PAD src0_sel:WORD_1 src1_sel:DWORD\n"
      "v_mfma_f32_32x32x16_bf16 v[16:31], v[128:131], v[160:163], v[16:31]\n"
      "ds_read_b128 v[168:171], v168 offset:61504\n"
      "ds_read_b64_tr_b16 v[140:141], v185 offset:3072\n"
      "ds_read_b64_tr_b16 v[142:143], v185 offset:3840\n"
      "s_mov_b32 m0, s72\n"
      "v_mfma_f32_32x32x16_bf16 v[32:47], v[132:135], v[156:159], v[32:47]\n"
      "ds_read_b64_tr_b16 v[144:145], v185 offset:3136\n"
      "ds_read_b64_tr_b16 v[146:147], v185 offset:3904\n"
      "ds_read_b64_tr_b16 v[148:149], v185 offset:3200\n"
      "ds_read_b64_tr_b16 v[150:151], v185 offset:3968\n"
      "v_mfma_f32_32x32x16_bf16 v[48:63], v[132:135], v[160:163], v[48:63]\n"
      "s_mov_b32 s40, 106496\n"
      "buffer_load_dwordx4 v203, s[44:47], s40 offen offset:0 lds\n"
      "buffer_load_dwordx4 v204, s[44:47], s40 offen offset:1024 lds\n"
      "v_mfma_f32_32x32x16_bf16 v[64:79], v[136:139], v[156:159], v[64:79]\n"
      "buffer_load_dwordx4 v205, s[44:47], s40 offen offset:2048 lds\n"
      "v_mfma_f32_32x32x16_bf16 v[80:95], v[136:139], v[160:163], v[80:95]\n"
      "s_cmp_lg_u32 %[wv], 0\n"
      "s_cbranch_scc1 142f\n"
      "ds_read_b64_tr_b16 v[152:153], v199 offset:0\n"
      "ds_read_b64_tr_b16 v[154:155], v199 offset:256\n"
      "v_mfma_f32_32x32x16_bf16 v[96:111], v[208:211], v[156:159], v[96:111]\n"
      "s_mov_b32 m0, s79\n"
      "s_mov_b32 s41, 159744\n"
      "buffer_load_dwordx4 v202, s[44:47], s41 offen lds\n"
      "v_mfma_f32_32x32x16_bf16 v[112:127], v[208:211], v[160:163], v[112:127]\n"
      "142:\n"
      "s_waitcnt vmcnt(6) lgkmcnt(0)\n"
      "v_mfma_f32_32x32x16_bf16 v[0:15], v[140:143], v[164:167], v[0:15]\n"
      "ds_read_b32 v184, v207 offset:48\n"
      "ds_read_b32 v198, v206 offset:48\n"
      "v_xor_b32_sdwa v156, v175, v200 dst_sel:DWORD dst_unused:UNUSED_PAD src0_sel:WORD_0 src1_sel:DWORD\n"
      "ds_read_b128 v[156:159], v156 offset:61504\n"
      "v_xor_b32_sdwa v160, v189, v200 dst_sel:DWORD dst_unused:UNUSED_PAD src0_sel:WORD_0 src1_sel:DWORD\n"
      "v_mfma_f32_32x32x16_bf16 v[16:31], v[140:143], v[168:171], v[16:31]\n"
      "ds_read_b128 v[160:163], v160 offset:61504\n"
      "ds_read_b64_tr_b16 v[128:129], v185 offset:6144\n"
      "ds_read_b64_tr_b16 v[130:131], v185 offset:6912\n"
      "s_mov_b32 m0, s73\n"
      "v_mfma_f32_32x32x16_bf16 v[32:47], v[144:147], v[164:167], v[32:47]\n"
      "ds_read_b64_tr_b16 v[132:133], v185 offset:6208\n"
      "ds_read_b64_tr_b16 v[134:135], v185 offset:6976\n"
      "ds_read_b64_tr_b16 v[136:137], v185 offset:6272\n"
      "ds_read_b64_tr_b16 v[138:139], v185 offset:7040\n"
      "v_mfma_f32_32x32x16_bf16 v[48:63], v[144:147], v[168:171], v[48:63]\n"
      "s_mov_b32 s40, 119808\n"
      "buffer_load_dwordx4 v203, s[44:47], s40 offen offset:0 lds\n"
      "buffer_load_dwordx4 v204, s[44:47], s40 offen offset:1024 lds\n"
      "v_mfma_f32_32x32x16_bf16 v[64:79], v[148:151], v[164:167], v[64:79]\n"
      "buffer_load_dwordx4 v205, s[44:47], s40 offen offset:2048 lds\n"
      "v_mfma_f32_32x32x16_bf16 v[80:95], v[148:151], v[168:171], v[80:95]\n"
      "s_cmp_lg_u32 %[wv], 1\n"
      "s_cbranch_scc1 152f\n"
      "ds_read_b64_tr_b16 v[152:153], v199 offset:0\n"
      "ds_read_b64_tr_b16 v[154:155], v199 offset:256\n"
      "v_mfma_f32_32x32x16_bf16 v[96:111], v[208:211], v[164:167], v[96:111]\n"
      "s_mov_b32 m0, s79\n"
      "s_mov_b32 s41, 173056\n"
      "buffer_load_dwordx4 v202, s[44:47], s41 offen lds\n"
      "v_mfma_f32_32x32x16_bf16 v[112:127], v[208:211], v[168:171], v[112:127]\n"
      "152:\n"
      "s_waitcnt vmcnt(6) lgkmcnt(0)\n"
      "v_mfma_f32_32x32x16_bf16 v[0:15], v[128:131], v[156:159], v[0:15]\n"
      "v_xor_b32_sdwa v164, v175, v200 dst_sel:DWORD dst_unused:UNUSED_PAD src0_sel:WORD_1 src1_sel:DWORD\n"
      "ds_read_b128 v[164:167], v164 offset:61504\n"
      "v_xor_b32_sdwa v168, v189, v200 dst_sel:DWORD dst_unused:UNUSED_PAD src0_sel:WORD_1 src1_sel:DWORD\n"
      "v_mfma_f32_32x32x16_bf16 v[16:31], v[128:131], v[160:163], v[16:31]\n"
      "ds_read_b128 v[168:171], v168 offset:61504\n"
      "ds_read_b64_tr_b16 v[140:141], v185 offset:9216\n"
      "ds_read_b64_tr_b16 v[142:143], v185 offset:9984\n"
      "s_mov_b32 m0, s74\n"
      "v_mfma_f32_32x32x16_bf16 v[32:47], v[132:135], v[156:159], v[32:47]\n"
      "ds_read_b64_tr_b16 v[144:145], v185 offset:9280\n"
      "ds_read_b64_tr_b16 v[146:147], v185 offset:10048\n"
      "ds_read_b64_tr_b16 v[148:149], v185 offset:9344\n"
      "ds_read_b64_tr_b16 v[150:151], v185 offset:10112\n"
      "v_mfma_f32_32x32x16_bf16 v[48:63], v[132:135], v[160:163], v[48:63]\n"
      "s_mov_b32 s40, 133120\n"
      "buffer_load_dwordx4 v203, s[44:47], s40 offen offset:0 lds\n"
      "buffer_load_dwordx4 v204, s[44:47], s40 offen offset:1024 lds\n"
      "v_mfma_f32_32x32x16_bf16 v[64:79], v[136:139], v[156:159], v[64:79]\n"
      "buffer_load_dwordx4 v205, s[44:47], s40 offen offset:2048 lds\n"
      "v_mfma_f32_32x32x16_bf16 v[80:95], v[136:139], v[160:163], v[80:95]\n"
      "s_cmp_lg_u32 %[wv], 2\n"
      "s_cbranch_scc1 162f\n"
      "ds_read_b64_tr_b16 v[152:153], v199 offset:0\n"
      "ds_read_b64_tr_b16 v[154:155], v199 offset:256\n"
      "v_mfma_f32_32x32x16_bf16 v[96:111], v[208:211], v[156:159], v[96:111]\n"
      "s_mov_b32 m0, s79\n"
      "s_mov_b32 s41, 186368\n"
      "buffer_load_dwordx4 v202, s[44:47], s41 offen lds\n"
      "v_mfma_f32_32x32x16_bf16 v[112:127], v[208:211], v[160:163], v[112:127]\n"
      "162:\n"
      "s_waitcnt vmcnt(6) lgkmcnt(0)\n"
      "v_mfma_f32_32x32x16_bf16 v[0:15], v[140:143], v[164:167], v[0:15]\n"
      "v_xor_b32_sdwa v156, v176, v200 dst_sel:DWORD dst_unused:UNUSED_PAD src0_sel:WORD_0 src1_sel:DWORD\n"
      "ds_read_b128 v[156:159], v156 offset:61504\n"
      "v_xor_b32_sdwa v160, v190, v200 dst_sel:DWORD dst_unused:UNUSED_PAD src0_sel:WORD_0 src1_sel:DWORD\n"
      "v_mfma_f32_32x32x16_bf16 v[16:31], v[140:143], v[168:171], v[16:31]\n"
      "ds_read_b128 v[160:163], v160 offset:61504\n"
      "ds_read_b64_tr_b16 v[128:129], v185 offset:0\n"
      "ds_read_b64_tr_b16 v[130:131], v185 offset:768\n"
      "s_mov_b32 m0, s75\n"
      "v_mfma_f32_32x32x16_bf16 v[32:47], v[144:147], v[164:167], v[32:47]\n"
      "ds_read_b64_tr_b16 v[132:133], v185 offset:64\n"
      "ds_read_b64_tr_b16 v[134:135], v185 offset:832\n"
      "ds_read_b64_tr_b16 v[136:137], v185 offset:128\n"
      "ds_read_b64_tr_b16 v[138:139], v185 offset:896\n"
      "v_mfma_f32_32x32x16_bf16 v[48:63], v[144:147], v[168:171], v[48:63]\n"
      "s_mov_b32 s40, 146432\n"
      "buffer_load_dwordx4 v203, s[44:47], s40 offen offset:0 lds\n"
      "buffer_load_dwordx4 v204, s[44:47], s40 offen offset:1024 lds\n"
      "v_mfma_f32_32x32x16_bf16 v[64:79], v[148:151], v[164:167], v[64:79]\n"
      "buffer_load_dwordx4 v205, s[44:47], s40 offen offset:2048 lds\n"
      "v_mfma_f32_32x32x16_bf16 v[80:95], v[148:151], v[168:171], v[80:95]\n"
      "s_cmp_lg_u32 %[wv], 3\n"
      "s_cbranch_scc1 172f\n"
      "ds_read_b64_tr_b16 v[152:153], v199 offset:0\n"
      "ds_read_b64_tr_b16 v[154:155], v199 offset:256\n"
      "v_mfma_f32_32x32x16_bf16 v[96:111], v[208:211], v[164:167], v[96:111]\n"
      "s_mov_b32 m0, s79\n"
      "s_mov_b32 s41, 199680\n"
      "buffer_load_dwordx4 v202, s[44:47], s41 offen lds\n"
      "v_mfma_f32_32x32x16_bf16 v[112:127], v[208:211], v[168:171], v[112:127]\n"
      "172:\n"
      "s_waitcnt vmcnt(6) lgkmcnt(0)\n"
      "v_mfma_f32_32x32x16_bf16 v[0:15], v[128:131], v[156:159], v[0:15]\n"
      "v_xor_b32_sdwa v164, v176, v200 dst_sel:DWORD dst_unused:UNUSED_PAD src0_sel:WORD_1 src1_sel:DWORD\n"
      "ds_read_b128 v[164:167], v164 offset:61504\n"
      "v_xor_b32_sdwa v168, v190, v200 dst_sel:DWORD dst_unused:UNUSED_PAD src0_sel:WORD_1 src1_sel:DWORD\n"
      "v_mfma_f32_32x32x16_bf16 v[16:31], v[128:131], v[160:163], v[16:31]\n"
      "ds_read_b128 v[168:171], v168 offset:61504\n"
      "ds_read_b64_tr_b16 v[140:141], v185 offset:3072\n"
      "ds_read_b64_tr_b16 v[142:143], v185 offset:3840\n"
      "s_mov_b32 m0, s72\n"
      "v_mfma_f32_32x32x16_bf16 v[32:47], v[132:135], v[156:159], v[32:47]\n"
      "ds_read_b64_tr_b16 v[144:145], v185 offset:3136\n"
      "ds_read_b64_tr_b16 v[146:147], v185 offset:3904\n"
      "ds_read_b64_tr_b16 v[148:149], v185 offset:3200\n"
      "ds_read_b64_tr_b16 v[150:151], v185 offset:3968\n"
      "v_mfma_f32_32x32x16_bf16 v[48:63], v[132:135], v[160:163], v[48:63]\n"
      "s_mov_b32 s40, 159744\n"
      "buffer_load_dwordx4 v203, s[44:47], s40 offen offset:0 lds\n"
      "buffer_load_dwordx4 v204, s[44:47], s40 offen offset:1024 lds\n"
      "v_mfma_f32_32x32x16_bf16 v[64:79], v[136:139], v[156:159], v[64:79]\n"
      "buffer_load_dwordx4 v205, s[44:47], s40 offen offset:2048 lds\n"
      "v_mfma_f32_32x32x16_bf16 v[80:95], v[136:139], v[160:163], v[80:95]\n"
      "s_cmp_lg_u32 %[wv], 0\n"
      "s_cbranch_scc1 182f\n"
      "ds_read_b64_tr_b16 v[208:209], v199 offset:1024\n"
      "ds_read_b64_tr_b16 v[210:211], v199 offset:1280\n"
      "v_mfma_f32_32x32x16_bf16 v[96:111], v[152:155], v[156:159], v[96:111]\n"
      "s_mov_b32 m0, s78\n"
      "s_mov_b32 s41, 212992\n"
      "buffer_load_dwordx4 v202, s[44:47], s41 offen lds\n"
      "v_mfma_f32_32x32x16_bf16 v[112:127], v[152:155], v[160:163], v[112:127]\n"
      "182:\n"
      "s_waitcnt vmcnt(6) lgkmcnt(0)\n"
      "v_mfma_f32_32x32x16_bf16 v[0:15], v[140:143], v[164:167], v[0:15]\n"
      "v_xor_b32_sdwa v156, v177, v200 dst_sel:DWORD dst_unused:UNUSED_PAD src0_sel:WORD_0 src1_sel:DWORD\n"
      "ds_read_b128 v[156:159], v156 offset:61504\n"
      "v_xor_b32_sdwa v160, v191, v200 dst_sel:DWORD dst_unused:UNUSED_PAD src0_sel:WORD_0 src1_sel:DWORD\n"
      "v_mfma_f32_32x32x16_bf16 v[16:31], v[140:143], v[168:171], v[16:31]\n"
      "ds_read_b128 v[160:163], v160 offset:61504\n"
      "ds_read_b64_tr_b16 v[128:129], v185 offset:6144\n"
      "ds_read_b64_tr_b16 v[130:131], v185 offset:6912\n"
      "s_mov_b32 m0, s73\n"
      "v_mfma_f32_32x32x16_bf16 v[32:47], v[144:147], v[164:167], v[32:47]\n"
      "ds_read_b64_tr_b16 v[132:133], v185 offset:6208\n"
      "ds_read_b64_tr_b16 v[134:135], v185 offset:6976\n"
      "ds_read_b64_tr_b16 v[136:137], v185 offset:6272\n"
      "ds_read_b64_tr_b16 v[138:139], v185 offset:7040\n"
      "v_mfma_f32_32x32x16_bf16 v[48:63], v[144:147], v[168:171], v[48:63]\n"
      "s_mov_b32 s40, 173056\n"
      "buffer_load_dwordx4 v203, s[44:47], s40 offen offset:0 lds\n"
      "buffer_load_dwordx4 v204, s[44:47], s40 offen offset:1024 lds\n"
      "v_mfma_f32_32x32x16_bf16 v[64:79], v[148:151], v[164:167], v[64:79]\n"
      "buffer_load_dwordx4 v205, s[44:47], s40 offen offset:2048 lds\n"
      "v_mfma_f32_32x32x16_bf16 v[80:95], v[148:151], v[168:171], v[80:95]\n"
      "s_cmp_lg_u32 %[wv], 1\n"
      "s_cbranch_scc1 192f\n"
      "ds_read_b64_tr_b16 v[208:209], v199 offset:1024\n"
      "ds_read_b64_tr_b16 v[210:211], v199 offset:1280\n"
      "v_mfma_f32_32x32x16_bf16 v[96:111], v[152:155], v[164:167], v[96:111]\n"
      "s_mov_b32 m0, s78\n"
      "s_mov_b32 s41, 226304\n"
      "buffer_load_dwordx4 v202, s[44:47], s41 offen lds\n"
      "v_mfma_f32_32x32x16_bf16 v[112:127], v[152:155], v[168:171], v[112:127]\n"
      "192:\n"
      "s_waitcnt vmcnt(6) lgkmcnt(0)\n"
      "v_mfma_f32_32x32x16_bf16 v[0:15], v[128:131], v[156:159], v[0:15]\n"
      "v_xor_b32_sdwa v164, v177, v200 dst_sel:DWORD dst_unused:UNUSED_PAD src0_sel:WORD_1 src1_sel:DWORD\n"
      "ds_read_b128 v[164:167], v164 offset:61504\n"
      "v_xor_b32_sdwa v168, v191, v200 dst_sel:DWORD dst_unused:UNUSED_PAD src0_sel:WORD_1 src1_sel:DWORD\n"
      "v_mfma_f32_32x32x16_bf16 v[16:31], v[128:131], v[160:163], v[16:31]\n"
      "ds_read_b128 v[168:171], v168 offset:61504\n"
      "ds_read_b64_tr_b16 v[140:141], v185 offset:9216\n"
      "ds_read_b64_tr_b16 v[142:143], v185 offset:9984\n"
      "s_mov_b32 m0, s74\n"
      "v_mfma_f32_32x32x16_bf16 v[32:47], v[132:135], v[156:159], v[32:47]\n"
      "ds_read_b64_tr_b16 v[144:145], v185 offset:9280\n"
      "ds_read_b64_tr_b16 v[146:147], v185 offset:10048\n"
      "ds_read_b64_tr_b16 v[148:149], v185 offset:9344\n"
      "ds_read_b64_tr_b16 v[150:151], v185 offset:10112\n"
      "v_mfma_f32_32x32x16_bf16 v[48:63], v[132:135], v[160:163], v[48:63]\n"
      "s_mov_b32 s40, 186368\n"
      "buffer_load_dwordx4 v203, s[44:47], s40 offen offset:0 lds\n"
      "buffer_load_dwordx4 v204, s[44:47], s40 offen offset:1024 lds\n"
      "v_mfma_f32_32x32x16_bf16 v[64:79], v[136:139], v[156:159], v[64:79]\n"
      "buffer_load_dwordx4 v205, s[44:47], s40 offen offset:2048 lds\n"
      "v_mfma_f32_32x32x16_bf16 v[80:95], v[136:139], v[160:163], v[80:95]\n"
      "s_cmp_lg_u32 %[wv], 2\n"
      "s_cbranch_scc1 202f\n"
      "ds_read_b64_tr_b16 v[208:209], v199 offset:1024\n"
      "ds_read_b64_tr_b16 v[210:211], v199 offset:1280\n"
      "v_mfma_f32_32x32x16_bf16 v[96:111], v[152:155], v[156:159], v[96:111]\n"
      "s_mov_b32 m0, s78\n"
      "s_mov_b32 s41, 239616\n"
      "buffer_load_dwordx4 v202, s[44:47], s41 offen lds\n"
      "v_mfma_f32_32x32x16_bf16 v[112:127], v[152:155], v[160:163], v[112:127]\n"
      "202:\n"
      "s_waitcnt vmcnt(6) lgkmcnt(0)\n"
      "v_mfma_f32_32x32x16_bf16 v[0:15], v[140:143], v[164:167], v[0:15]\n"
      "v_xor_b32_sdwa v156, v178, v200 dst_sel:DWORD dst_unused:UNUSED_PAD src0_sel:WORD_0 src1_sel:DWORD\n"
      "ds_read_b128 v[156:159], v156 offset:61504\n"
      "v_xor_b32_sdwa v160, v192, v200 dst_sel:DWORD dst_unused:UNUSED_PAD src0_sel:WORD_0 src1_sel:DWORD\n"
      "v_mfma_f32_32x32x16_bf16 v[16:31], v[140:143], v[168:171], v[16:31]\n"
      "ds_read_b128 v[160:163], v160 offset:61504\n"
      "ds_read_b64_tr_b16 v[128:129], v185 offset:0\n"
      "ds_read_b64_tr_b16 v[130:131], v185 offset:768\n"
      "s_mov_b32 m0, s75\n"
      "v_mfma_f32_32x32x16_bf16 v[32:47], v[144:147], v[164:167], v[32:47]\n"
      "ds_read_b64_tr_b16 v[132:133], v185 offset:64\n"
      "ds_read_b64_tr_b16 v[134:135], v185 offset:832\n"
      "ds_read_b64_tr_b16 v[136:137], v185 offset:128\n"
      "ds_read_b64_tr_b16 v[138:139], v185 offset:896\n"
      "v_mfma_f32_32x32x16_bf16 v[48:63], v[144:147], v[168:171], v[48:63]\n"
      "s_mov_b32 s40, 199680\n"
      "buffer_load_dwordx4 v203, s[44:47], s40 offen offset:0 lds\n"
      "buffer_load_dwordx4 v204, s[44:47], s40 offen offset:1024 lds\n"
      "v_mfma_f32_32x32x16_bf16 v[64:79], v[148:151], v[164:167], v[64:79]\n"
      "buffer_load_dwordx4 v205, s[44:47], s40 offen offset:2048 lds\n"
      "v_mfma_f32_32x32x16_bf16 v[80:95], v[148:151], v[168:171], v[80:95]\n"
      "s_cmp_lg_u32 %[wv], 3\n"
      "s_cbranch_scc1 212f\n"
      "ds_read_b64_tr_b16 v[208:209], v199 offset:1024\n"
      "ds_read_b64_tr_b16 v[210:211], v199 offset:1280\n"
      "v_mfma_f32_32x32x16_bf16 v[96:111], v[152:155], v[164:167], v[96:111]\n"
      "s_mov_b32 m0, s78\n"
      "s_mov_b32 s41, 252928\n"
      "buffer_load_dwordx4 v202, s[44:47], s41 offen lds\n"
      "v_mfma_f32_32x32x16_bf16 v[112:127], v[152:155], v[168:171], v[112:127]\n"
      "212:\n"
      "s_waitcnt vmcnt(6) lgkmcnt(0)\n"
      "v_mfma_f32_32x32x16_bf16 v[0:15], v[128:131], v[156:159], v[0:15]\n"
      "v_xor_b32_sdwa v164, v178, v200 dst_sel:DWORD dst_unused:UNUSED_PAD src0_sel:WORD_1 src1_sel:DWORD\n"
      "ds_read_b128 v[164:167], v164 offset:61504\n"
      "v_xor_b32_sdwa v168, v192, v200 dst_sel:DWORD dst_unused:UNUSED_PAD src0_sel:WORD_1 src1_sel:DWORD\n"
      "v_mfma_f32_32x32x16_bf16 v[16:31], v[128:131], v[160:163], v[16:31]\n"
      "ds_read_b128 v[168:171], v168 offset:61504\n"
      "ds_read_b64_tr_b16 v[140:141], v185 offset:3072\n"
      "ds_read_b64_tr_b16 v[142:143], v185 offset:3840\n"
      "s_mov_b32 m0, s72\n"
      "v_mfma_f32_32x32x16_bf16 v[32:47], v[132:135], v[156:159], v[32:47]\n"
      "ds_read_b64_tr_b16 v[144:145], v185 offset:3136\n"
      "ds_read_b64_tr_b16 v[146:147], v185 offset:3904\n"
      "ds_read_b64_tr_b16 v[148:149], v185 offset:3200\n"
      "ds_read_b64_tr_b16 v[150:151], v185 offset:3968\n"
      "v_mfma_f32_32x32x16_bf16 v[48:63], v[132:135], v[160:163], v[48:63]\n"
      "s_mov_b32 s40, 212992\n"
      "buffer_load_dwordx4 v203, s[44:47], s40 offen offset:0 lds\n"
      "buffer_load_dwordx4 v204, s[44:47], s40 offen offset:1024 lds\n"
      "v_mfma_f32_32x32x16_bf16 v[64:79], v[136:139], v[156:159], v[64:79]\n"
      "buffer_load_dwordx4 v205, s[44:47], s40 offen offset:2048 lds\n"
      "v_mfma_f32_32x32x16_bf16 v[80:95], v[136:139], v[160:163], v[80:95]\n"
      "s_cmp_lg_u32 %[wv], 0\n"
      "s_cbranch_scc1 222f\n"
      "ds_read_b64_tr_b16 v[152:153], v199 offset:0\n"
      "ds_read_b64_tr_b16 v[154:155], v199 offset:256\n"
      "v_mfma_f32_32x32x16_bf16 v[96:111], v[208:211], v[156:159], v[96:111]\n"
      "s_mov_b32 m0, s79\n"
      "s_mov_b32 s41, 266240\n"
      "buffer_load_dwordx4 v202, s[44:47], s41 offen lds\n"
      "v_mfma_f32_32x32x16_bf16 v[112:127], v[208:211], v[160:163], v[112:127]\n"
      "222:\n"
      "s_waitcnt vmcnt(6) lgkmcnt(0)\n"
      "v_mfma_f32_32x32x16_bf16 v[0:15], v[140:143], v[164:167], v[0:15]\n"
      "v_xor_b32_sdwa v156, v179, v200 dst_sel:DWORD dst_unused:UNUSED_PAD src0_sel:WORD_0 src1_sel:DWORD\n"
      "ds_read_b128 v[156:159], v156 offset:61504\n"
      "v_xor_b32_sdwa v160, v193, v200 dst_sel:DWORD dst_unused:UNUSED_PAD src0_sel:WORD_0 src1_sel:DWORD\n"
      "v_mfma_f32_32x32x16_bf16 v[16:31], v[140:143], v[168:171], v[16:31]\n"
      "ds_read_b128 v[160:163], v160 offset:61504\n"
      "ds_read_b64_tr_b16 v[128:129], v185 offset:6144\n"
      "ds_read_b64_tr_b16 v[130:131], v185 offset:6912\n"
      "s_mov_b32 m0, s73\n"
      "v_mfma_f32_32x32x16_bf16 v[32:47], v[144:147], v[164:167], v[32:47]\n"
      "ds_read_b64_tr_b16 v[132:133], v185 offset:6208\n"
      "ds_read_b64_tr_b16 v[134:135], v185 offset:6976\n"
      "ds_read_b64_tr_b16 v[136:137], v185 offset:6272\n"
      "ds_read_b64_tr_b16 v[138:139], v185 offset:7040\n"
      "v_mfma_f32_32x32x16_bf16 v[48:63], v[144:147], v[168:171], v[48:63]\n"
      "s_mov_b32 s40, 226304\n"
      "buffer_load_dwordx4 v203, s[44:47], s40 offen offset:0 lds\n"
      "buffer_load_dwordx4 v204, s[44:47], s40 offen offset:1024 lds\n"
      "v_mfma_f32_32x32x16_bf16 v[64:79], v[148:151], v[164:167], v[64:79]\n"
      "buffer_load_dwordx4 v205, s[44:47], s40 offen offset:2048 lds\n"
      "v_mfma_f32_32x32x16_bf16 v[80:95], v[148:151], v[168:171], v[80:95]\n"
      "s_cmp_lg_u32 %[wv], 1\n"
      "s_cbranch_scc1 232f\n"
      "ds_read_b64_tr_b16 v[152:153], v199 offset:0\n"
      "ds_read_b64_tr_b16 v[154:155], v199 offset:256\n"
      "v_mfma_f32_32x32x16_bf16 v[96:111], v[208:211], v[164:167], v[96:111]\n"
      "s_mov_b32 m0, s79\n"
      "s_mov_b32 s41, 279552\n"
      "buffer_load_dwordx4 v202, s[44:47], s41 offen lds\n"
      "v_mfma_f32_32x32x16_bf16 v[112:127], v[208:211], v[168:171], v[112:127]\n"
      "232:\n"
      "s_waitcnt vmcnt(6) lgkmcnt(0)\n"
      "v_mfma_f32_32x32x16_bf16 v[0:15], v[128:131], v[156:159], v[0:15]\n"
      "v_xor_b32_sdwa v164, v179, v200 dst_sel:DWORD dst_unused:UNUSED_PAD src0_sel:WORD_1 src1_sel:DWORD\n"
      "ds_read_b128 v[164:167], v164 offset:61504\n"
      "v_xor_b32_sdwa v168, v193, v200 dst_sel:DWORD dst_unused:UNUSED_PAD src0_sel:WORD_1 src1_sel:DWORD\n"
      "v_mfma_f32_32x32x16_bf16 v[16:31], v[128:131], v[160:163], v[16:31]\n"
      "ds_read_b128 v[168:171], v168 offset:61504\n"
      "ds_read_b64_tr_b16 v[140:141], v185 offset:9216\n"
      "ds_read_b64_tr_b16 v[142:143], v185 offset:9984\n"
      "s_mov_b32 m0, s74\n"
      "v_mfma_f32_32x32x16_bf16 v[32:47], v[132:135], v[156:159], v[32:47]\n"
      "ds_read_b64_tr_b16 v[144:145], v185 offset:9280\n"
      "ds_read_b64_tr_b16 v[146:147], v185 offset:10048\n"
      "ds_read_b64_tr_b16 v[148:149], v185 offset:9344\n"
      "ds_read_b64_tr_b16 v[150:151], v185 offset:10112\n"
      "v_mfma_f32_32x32x16_bf16 v[48:63], v[132:135], v[160:163], v[48:63]\n"
      "s_mov_b32 s40, 239616\n"
      "buffer_load_dwordx4 v203, s[44:47], s40 offen offset:0 lds\n"
      "buffer_load_dwordx4 v204, s[44:47], s40 offen offset:1024 lds\n"
      "v_mfma_f32_32x32x16_bf16 v[64:79], v[136:139], v[156:159], v[64:79]\n"
      "buffer_load_dwordx4 v205, s[44:47], s40 offen offset:2048 lds\n"
      "v_mfma_f32_32x32x16_bf16 v[80:95], v[136:139], v[160:163], v[80:95]\n"
      "s_cmp_lg_u32 %[wv], 2\n"
      "s_cbranch_scc1 242f\n"
      "ds_read_b64_tr_b16 v[152:153], v199 offset:0\n"
      "ds_read_b64_tr_b16 v[154:155], v199 offset:256\n"
      "v_mfma_f32_32x32x16_bf16 v[96:111], v[208:211], v[156:159], v[96:111]\n"
      "s_mov_b32 m0, s79\n"
      "s_mov_b32 s41, 292864\n"
      "buffer_load_dwordx4 v202, s[44:47], s41 offen lds\n"
      "v_mfma_f32_32x32x16_bf16 v[112:127], v[208:211], v[160:163], v[112:127]\n"
      "242:\n"
      "s_waitcnt vmcnt(6) lgkmcnt(0)\n"
      "v_mfma_f32_32x32x16_bf16 v[0:15], v[140:143], v[164:167], v[0:15]\n"
      "v_xor_b32_sdwa v156, v180, v200 dst_sel:DWORD dst_unused:UNUSED_PAD src0_sel:WORD_0 src1_sel:DWORD\n"
      "ds_read_b128 v[156:159], v156 offset:61504\n"
      "v_xor_b32_sdwa v160, v194, v200 dst_sel:DWORD dst_unused:UNUSED_PAD src0_sel:WORD_0 src1_sel:DWORD\n"
      "v_mfma_f32_32x32x16_bf16 v[16:31], v[140:143], v[168:171], v[16:31]\n"
      "ds_read_b128 v[160:163], v160 offset:61504\n"
      "ds_read_b64_tr_b16 v[128:129], v185 offset:0\n"
      "ds_read_b64_tr_b16 v[130:131], v185 offset:768\n"
      "s_mov_b32 m0, s75\n"
      "v_mfma_f32_32x32x16_bf16 v[32:47], v[144:147], v[164:167], v[32:47]\n"
      "ds_read_b64_tr_b16 v[132:133], v185 offset:64\n"
      "ds_read_b64_tr_b16 v[134:135], v185 offset:832\n"
      "ds_read_b64_tr_b16 v[136:137], v185 offset:128\n"
      "ds_read_b64_tr_b16 v[138:139], v185 offset:896\n"
      "v_mfma_f32_32x32x16_bf16 v[48:63], v[144:147], v[168:171], v[48:63]\n"
      "s_mov_b32 s40, 252928\n"
      "buffer_load_dwordx4 v203, s[44:47], s40 offen offset:0 lds\n"
      "buffer_load_dwordx4 v204, s[44:47], s40 offen offset:1024 lds\n"
      "v_mfma_f32_32x32x16_bf16 v[64:79], v[148:151], v[164:167], v[64:79]\n"
      "buffer_load_dwordx4 v205, s[44:47], s40 offen offset:2048 lds\n"
      "v_mfma_f32_32x32x16_bf16 v[80:95], v[148:151], v[168:171], v[80:95]\n"
      "s_cmp_lg_u32 %[wv], 3\n"
      "s_cbranch_scc1 252f\n"
      "ds_read_b64_tr_b16 v[152:153], v199 offset:0\n"
      "ds_read_b64_tr_b16 v[154:155], v199 offset:256\n"
      "v_mfma_f32_32x32x16_bf16 v[96:111], v[208:211], v[164:167], v[96:111]\n"
      "s_mov_b32 m0, s79\n"
      "s_mov_b32 s41, 306176\n"
      "buffer_load_dwordx4 v202, s[44:47], s41 offen lds\n"
      "v_mfma_f32_32x32x16_bf16 v[112:127], v[208:211], v[168:171], v[112:127]\n"
      "252:\n"
      "s_waitcnt vmcnt(6) lgkmcnt(0)\n"
      "v_mfma_f32_32x32x16_bf16 v[0:15], v[128:131], v[156:159], v[0:15]\n"
      "v_xor_b32_sdwa v164, v180, v200 dst_sel:DWORD dst_unused:UNUSED_PAD src0_sel:WORD_1 src1_sel:DWORD\n"
      "ds_read_b128 v[164:167], v164 offset:61504\n"
      "v_xor_b32_sdwa v168, v194, v200 dst_sel:DWORD dst_unused:UNUSED_PAD src0_sel:WORD_1 src1_sel:DWORD\n"
      "v_mfma_f32_32x32x16_bf16 v[16:31], v[128:131], v[160:163], v[16:31]\n"
      "ds_read_b128 v[168:171], v168 offset:61504\n"
      "ds_read_b64_tr_b16 v[140:141], v185 offset:3072\n"
      "ds_read_b64_tr_b16 v[142:143], v185 offset:3840\n"
      "s_mov_b32 m0, s72\n"
      "v_mfma_f32_32x32x16_bf16 v[32:47], v[132:135], v[156:159], v[32:47]\n"
      "ds_read_b64_tr_b16 v[144:145], v185 offset:3136\n"
      "ds_read_b64_tr_b16 v[146:147], v185 offset:3904\n"
      "ds_read_b64_tr_b16 v[148:149], v185 offset:3200\n"
      "ds_read_b64_tr_b16 v[150:151], v185 offset:3968\n"
      "v_mfma_f32_32x32x16_bf16 v[48:63], v[132:135], v[160:163], v[48:63]\n"
      "s_mov_b32 s40, 266240\n"
      "buffer_load_dwordx4 v203, s[44:47], s40 offen offset:0 lds\n"
      "buffer_load_dwordx4 v204, s[44:47], s40 offen offset:1024 lds\n"
      "v_mfma_f32_32x32x16_bf16 v[64:79], v[136:139], v[156:159], v[64:79]\n"
      "buffer_load_dwordx4 v205, s[44:47], s40 offen offset:2048 lds\n"
      "v_mfma_f32_32x32x16_bf16 v[80:95], v[136:139], v[160:163], v[80:95]\n"
      "s_cmp_lg_u32 %[wv], 0\n"
      "s_cbranch_scc1 262f\n"
      "ds_read_b64_tr_b16 v[208:209], v199 offset:1024\n"
      "ds_read_b64_tr_b16 v[210:211], v199 offset:1280\n"
      "v_mfma_f32_32x32x16_bf16 v[96:111], v[152:155], v[156:159], v[96:111]\n"
      "s_mov_b32 m0, s78\n"
      "s_mov_b32 s41, 319488\n"
      "buffer_load_dwordx4 v202, s[44:47], s41 offen lds\n"
      "v_mfma_f32_32x32x16_bf16 v[112:127], v[152:155], v[160:163], v[112:127]\n"
      "262:\n"
      "s_waitcnt vmcnt(6) lgkmcnt(0)\n"
      "v_mfma_f32_32x32x16_bf16 v[0:15], v[140:143], v[164:167], v[0:15]\n"
      "v_xor_b32_sdwa v156, v181, v200 dst_sel:DWORD dst_unused:UNUSED_PAD src0_sel:WORD_0 src1_sel:DWORD\n"
      "ds_read_b128 v[156:159], v156 offset:61504\n"
      "v_xor_b32_sdwa v160, v195, v200 dst_sel:DWORD dst_unused:UNUSED_PAD src0_sel:WORD_0 src1_sel:DWORD\n"
      "v_mfma_f32_32x32x16_bf16 v[16:31], v[140:143], v[168:171], v[16:31]\n"
      "ds_read_b128 v[160:163], v160 offset:61504\n"
      "ds_read_b64_tr_b16 v[128:129], v185 offset:6144\n"
      "ds_read_b64_tr_b16 v[130:131], v185 offset:6912\n"
      "s_mov_b32 m0, s73\n"
      "v_mfma_f32_32x32x16_bf16 v[32:47], v[144:147], v[164:167], v[32:47]\n"
      "ds_read_b64_tr_b16 v[132:133], v185 offset:6208\n"
      "ds_read_b64_tr_b16 v[134:135], v185 offset:6976\n"
      "ds_read_b64_tr_b16 v[136:137], v185 offset:6272\n"
      "ds_read_b64_tr_b16 v[138:139], v185 offset:7040\n"
      "v_mfma_f32_32x32x16_bf16 v[48:63], v[144:147], v[168:171], v[48:63]\n"
      "s_mov_b32 s40, 279552\n"
      "buffer_load_dwordx4 v203, s[44:47], s40 offen offset:0 lds\n"
      "buffer_load_dwordx4 v204, s[44:47], s40 offen offset:1024 lds\n"
      "v_mfma_f32_32x32x16_bf16 v[64:79], v[148:151], v[164:167], v[64:79]\n"
      "buffer_load_dwordx4 v205, s[44:47], s40 offen offset:2048 lds\n"
      "v_mfma_f32_32x32x16_bf16 v[80:95], v[148:151], v[168:171], v[80:95]\n"
      "s_cmp_lg_u32 %[wv], 1\n"
      "s_cbranch_scc1 272f\n"
      "ds_read_b64_tr_b16 v[208:209], v199 offset:1024\n"
      "ds_read_b64_tr_b16 v[210:211], v199 offset:1280\n"
      "v_mfma_f32_32x32x16_bf16 v[96:111], v[152:155], v[164:167], v[96:111]\n"
      "s_mov_b32 m0, s78\n"
      "s_mov_b32 s41, 332800\n"
      "buffer_load_dwordx4 v202, s[44:47], s41 offen lds\n"
      "v_mfma_f32_32x32x16_bf16 v[112:127], v[152:155], v[168:171], v[112:127]\n"
      "272:\n"
      "s_waitcnt vmcnt(6) lgkmcnt(0)\n"
      "v_mfma_f32_32x32x16_bf16 v[0:15], v[128:131], v[156:159], v[0:15]\n"
      "v_xor_b32_sdwa v164, v181, v200 dst_sel:DWORD dst_unused:UNUSED_PAD src0_sel:WORD_1 src1_sel:DWORD\n"
      "ds_read_b128 v[164:167], v164 offset:61504\n"
      "v_xor_b32_sdwa v168, v195, v200 dst_sel:DWORD dst_unused:UNUSED_PAD src0_sel:WORD_1 src1_sel:DWORD\n"
      "v_mfma_f32_32x32x16_bf16 v[16:31], v[128:131], v[160:163], v[16:31]\n"
      "ds_read_b128 v[168:171], v168 offset:61504\n"
      "ds_read_b64_tr_b16 v[140:141], v185 offset:9216\n"
      "ds_read_b64_tr_b16 v[142:143], v185 offset:9984\n"
      "s_mov_b32 m0, s74\n"
      "v_mfma_f32_32x32x16_bf16 v[32:47], v[132:135], v[156:159], v[32:47]\n"
      "ds_read_b64_tr_b16 v[144:145], v185 offset:9280\n"
      "ds_read_b64_tr_b16 v[146:147], v185 offset:10048\n"
      "ds_read_b64_tr_b16 v[148:149], v185 offset:9344\n"
      "ds_read_b64_tr_b16 v[150:151], v185 offset:10112\n"
      "v_mfma_f32_32x32x16_bf16 v[48:63], v[132:135], v[160:163], v[48:63]\n"
      "s_mov_b32 s40, 292864\n"
      "buffer_load_dwordx4 v203, s[44:47], s40 offen offset:0 lds\n"
      "buffer_load_dwordx4 v204, s[44:47], s40 offen offset:1024 lds\n"
      "v_mfma_f32_32x32x16_bf16 v[64:79], v[136:139], v[156:159], v[64:79]\n"
      "buffer_load_dwordx4 v205, s[44:47], s40 offen offset:2048 lds\n"
      "v_mfma_f32_32x32x16_bf16 v[80:95], v[136:139], v[160:163], v[80:95]\n"
      "s_cmp_lg_u32 %[wv], 2\n"
      "s_cbranch_scc1 282f\n"
      "ds_read_b64_tr_b16 v[208:209], v199 offset:1024\n"
      "ds_read_b64_tr_b16 v[210:211], v199 offset:1280\n"
      "v_mfma_f32_32x32x16_bf16 v[96:111], v[152:155], v[156:159], v[96:111]\n"
      "v_mfma_f32_32x32x16_bf16 v[112:127], v[152:155], v[160:163], v[112:127]\n"
      "282:\n"
      "s_waitcnt vmcnt(6) lgkmcnt(0)\n"
      "v_mfma_f32_32x32x16_bf16 v[0:15], v[140:143], v[164:167], v[0:15]\n"
      "v_xor_b32_sdwa v156, v182, v200 dst_sel:DWORD dst_unused:UNUSED_PAD src0_sel:WORD_0 src1_sel:DWORD\n"
      "ds_read_b128 v[156:159], v156 offset:61504\n"
      "v_xor_b32_sdwa v160, v196, v200 dst_sel:DWORD dst_unused:UNUSED_PAD src0_sel:WORD_0 src1_sel:DWORD\n"
      "v_mfma_f32_32x32x16_bf16 v[16:31], v[140:143], v[168:171], v[16:31]\n"
      "ds_read_b128 v[160:163], v160 offset:61504\n"
      "ds_read_b64_tr_b16 v[128:129], v185 offset:0\n"
      "ds_read_b64_tr_b16 v[130:131], v185 offset:768\n"
      "s_mov_b32 m0, s75\n"
      "v_mfma_f32_32x32x16_bf16 v[32:47], v[144:147], v[164:167], v[32:47]\n"
      "ds_read_b64_tr_b16 v[132:133], v185 offset:64\n"
      "ds_read_b64_tr_b16 v[134:135], v185 offset:832\n"
      "ds_read_b64_tr_b16 v[136:137], v185 offset:128\n"
      "ds_read_b64_tr_b16 v[138:139], v185 offset:896\n"
      "v_mfma_f32_32x32x16_bf16 v[48:63], v[144:147], v[168:171], v[48:63]\n"
      "s_mov_b32 s40, 306176\n"
      "buffer_load_dwordx4 v203, s[44:47], s40 offen offset:0 lds\n"
      "buffer_load_dwordx4 v204, s[44:47], s40 offen offset:1024 lds\n"
      "v_mfma_f32_32x32x16_bf16 v[64:79], v[148:151], v[164:167], v[64:79]\n"
      "buffer_load_dwordx4 v205, s[44:47], s40 offen offset:2048 lds\n"
      "v_mfma_f32_32x32x16_bf16 v[80:95], v[148:151], v[168:171], v[80:95]\n"
      "s_cmp_lg_u32 %[wv], 3\n"
      "s_cbranch_scc1 292f\n"
      "ds_read_b64_tr_b16 v[208:209], v199 offset:1024\n"
      "ds_read_b64_tr_b16 v[210:211], v199 offset:1280\n"
      "v_mfma_f32_32x32x16_bf16 v[96:111], v[152:155], v[164:167], v[96:111]\n"
      "v_mfma_f32_32x32x16_bf16 v[112:127], v[152:155], v[168:171], v[112:127]\n"
      "292:\n"
      "s_waitcnt vmcnt(6) lgkmcnt(0)\n"
      "v_mfma_f32_32x32x16_bf16 v[0:15], v[128:131], v[156:159], v[0:15]\n"
      "v_xor_b32_sdwa v164, v182, v200 dst_sel:DWORD dst_unused:UNUSED_PAD src0_sel:WORD_1 src1_sel:DWORD\n"
      "ds_read_b128 v[164:167], v164 offset:61504\n"
      "v_xor_b32_sdwa v168, v196, v200 dst_sel:DWORD dst_unused:UNUSED_PAD src0_sel:WORD_1 src1_sel:DWORD\n"
      "v_mfma_f32_32x32x16_bf16 v[16:31], v[128:131], v[160:163], v[16:31]\n"
      "ds_read_b128 v[168:171], v168 offset:61504\n"
      "ds_read_b64_tr_b16 v[140:141], v185 offset:3072\n"
      "ds_read_b64_tr_b16 v[142:143], v185 offset:3840\n"
      "s_mov_b32 m0, s72\n"
      "v_mfma_f32_32x32x16_bf16 v[32:47], v[132:135], v[156:159], v[32:47]\n"
      "ds_read_b64_tr_b16 v[144:145], v185 offset:3136\n"
      "ds_read_b64_tr_b16 v[146:147], v185 offset:3904\n"
      "ds_read_b64_tr_b16 v[148:149], v185 offset:3200\n"
      "ds_read_b64_tr_b16 v[150:151], v185 offset:3968\n"
      "v_mfma_f32_32x32x16_bf16 v[48:63], v[132:135], v[160:163], v[48:63]\n"
      "s_mov_b32 s40, 319488\n"
      "buffer_load_dwordx4 v203, s[44:47], s40 offen offset:0 lds\n"
      "buffer_load_dwordx4 v204, s[44:47], s40 offen offset:1024 lds\n"
      "v_mfma_f32_32x32x16_bf16 v[64:79], v[136:139], v[156:159], v[64:79]\n"
      "buffer_load_dwordx4 v205, s[44:47], s40 offen offset:2048 lds\n"
      "v_mfma_f32_32x32x16_bf16 v[80:95], v[136:139], v[160:163], v[80:95]\n"
      "s_cmp_lg_u32 %[wv], 0\n"
      "s_cbranch_scc1 302f\n"
      "ds_read_b64_tr_b16 v[152:153], v199 offset:0\n"
      "ds_read_b64_tr_b16 v[154:155], v199 offset:256\n"
      "v_mfma_f32_32x32x16_bf16 v[96:111], v[208:211], v[156:159], v[96:111]\n"
      "v_mfma_f32_32x32x16_bf16 v[112:127], v[208:211], v[160:163], v[112:127]\n"
      "302:\n"
      "s_waitcnt vmcnt(6) lgkmcnt(0)\n"
      "v_mfma_f32_32x32x16_bf16 v[0:15], v[140:143], v[164:167], v[0:15]\n"
      "v_xor_b32_sdwa v156, v183, v200 dst_sel:DWORD dst_unused:UNUSED_PAD src0_sel:WORD_0 src1_sel:DWORD\n"
      "ds_read_b128 v[156:159], v156 offset:61504\n"
      "v_xor_b32_sdwa v160, v197, v200 dst_sel:DWORD dst_unused:UNUSED_PAD src0_sel:WORD_0 src1_sel:DWORD\n"
      "v_mfma_f32_32x32x16_bf16 v[16:31], v[140:143], v[168:171], v[16:31]\n"
      "ds_read_b128 v[160:163], v160 offset:61504\n"
      "ds_read_b64_tr_b16 v[128:129], v185 offset:6144\n"
      "ds_read_b64_tr_b16 v[130:131], v185 offset:6912\n"
      "s_mov_b32 m0, s73\n"
      "v_mfma_f32_32x32x16_bf16 v[32:47], v[144:147], v[164:167], v[32:47]\n"
      "ds_read_b64_tr_b16 v[132:133], v185 offset:6208\n"
      "ds_read_b64_tr_b16 v[134:135], v185 offset:6976\n"
      "ds_read_b64_tr_b16 v[136:137], v185 offset:6272\n"
      "ds_read_b64_tr_b16 v[138:139], v185 offset:7040\n"
      "v_mfma_f32_32x32x16_bf16 v[48:63], v[144:147], v[168:171], v[48:63]\n"
      "s_mov_b32 s40, 332800\n"
      "buffer_load_dwordx4 v203, s[44:47], s40 offen offset:0 lds\n"
      "buffer_load_dwordx4 v204, s[44:47], s40 offen offset:1024 lds\n"
      "v_mfma_f32_32x32x16_bf16 v[64:79], v[148:151], v[164:167], v[64:79]\n"
      "buffer_load_dwordx4 v205, s[44:47], s40 offen offset:2048 lds\n"
      "v_mfma_f32_32x32x16_bf16 v[80:95], v[148:151], v[168:171], v[80:95]\n"
      "s_cmp_lg_u32 %[wv], 1\n"
      "s_cbranch_scc1 312f\n"
      "ds_read_b64_tr_b16 v[152:153], v199 offset:0\n"
      "ds_read_b64_tr_b16 v[154:155], v199 offset:256\n"
      "v_mfma_f32_32x32x16_bf16 v[96:111], v[208:211], v[164:167], v[96:111]\n"
      "v_mfma_f32_32x32x16_bf16 v[112:127], v[208:211], v[168:171], v[112:127]\n"
      "312:\n"
      "s_waitcnt vmcnt(6) lgkmcnt(0)\n"
      "v_mfma_f32_32x32x16_bf16 v[0:15], v[128:131], v[156:159], v[0:15]\n"
      "v_xor_b32_sdwa v164, v183, v200 dst_sel:DWORD dst_unused:UNUSED_PAD src0_sel:WORD_1 src1_sel:DWORD\n"
      "ds_read_b128 v[164:167], v164 offset:61504\n"
      "v_xor_b32_sdwa v168, v197, v200 dst_sel:DWORD dst_unused:UNUSED_PAD src0_sel:WORD_1 src1_sel:DWORD\n"
      "v_mfma_f32_32x32x16_bf16 v[16:31], v[128:131], v[160:163], v[16:31]\n"
      "ds_read_b128 v[168:171], v168 offset:61504\n"
      "ds_read_b64_tr_b16 v[140:141], v185 offset:9216\n"
      "ds_read_b64_tr_b16 v[142:143], v185 offset:9984\n"
      "v_mfma_f32_32x32x16_bf16 v[32:47], v[132:135], v[156:159], v[32:47]\n"
      "ds_read_b64_tr_b16 v[144:145], v185 offset:9280\n"
      "ds_read_b64_tr_b16 v[146:147], v185 offset:10048\n"
      "ds_read_b64_tr_b16 v[148:149], v185 offset:9344\n"
      "ds_read_b64_tr_b16 v[150:151], v185 offset:10112\n"
      "v_mfma_f32_32x32x16_bf16 v[48:63], v[132:135], v[160:163], v[48:63]\n"
      "v_mfma_f32_32x32x16_bf16 v[64:79], v[136:139], v[156:159], v[64:79]\n"
      "v_mfma_f32_32x32x16_bf16 v[80:95], v[136:139], v[160:163], v[80:95]\n"
      "s_cmp_lg_u32 %[wv], 2\n"
      "s_cbranch_scc1 322f\n"
      "v_mfma_f32_32x32x16_bf16 v[96:111], v[208:211], v[156:159], v[96:111]\n"
      "v_mfma_f32_32x32x16_bf16 v[112:127], v[208:211], v[160:163], v[112:127]\n"
      "322:\n"
      "s_waitcnt vmcnt(3) lgkmcnt(0)\n"
      "v_mfma_f32_32x32x16_bf16 v[0:15], v[140:143], v[164:167], v[0:15]\n"
      "v_xor_b32_sdwa v156, v184, v200 dst_sel:DWORD dst_unused:UNUSED_PAD src0_sel:WORD_0 src1_sel:DWORD\n"
      "ds_read_b128 v[156:159], v156 offset:61504\n"
      "v_xor_b32_sdwa v160, v198, v200 dst_sel:DWORD dst_unused:UNUSED_PAD src0_sel:WORD_0 src1_sel:DWORD\n"
      "v_mfma_f32_32x32x16_bf16 v[16:31], v[140:143], v[168:171], v[16:31]\n"
      "ds_read_b128 v[160:163], v160 offset:61504\n"
      "ds_read_b64_tr_b16 v[128:129], v185 offset:0\n"
      "ds_read_b64_tr_b16 v[130:131], v185 offset:768\n"
      "v_mfma_f32_32x32x16_bf16 v[32:47], v[144:147], v[164:167], v[32:47]\n"
      "ds_read_b64_tr_b16 v[132:133], v185 offset:64\n"
      "ds_read_b64_tr_b16 v[134:135], v185 offset:832\n"
      "ds_read_b64_tr_b16 v[136:137], v185 offset:128\n"
      "ds_read_b64_tr_b16 v[138:139], v185 offset:896\n"
      "v_mfma_f32_32x32x16_bf16 v[48:63], v[144:147], v[168:171], v[48:63]\n"
      "v_mfma_f32_32x32x16_bf16 v[64:79], v[148:151], v[164:167], v[64:79]\n"
      "v_mfma_f32_32x32x16_bf16 v[80:95], v[148:151], v[168:171], v[80:95]\n"
      "s_cmp_lg_u32 %[wv], 3\n"
      "s_cbranch_scc1 332f\n"
      "v_mfma_f32_32x32x16_bf16 v[96:111], v[208:211], v[164:167], v[96:111]\n"
      "v_mfma_f32_32x32x16_bf16 v[112:127], v[208:211], v[168:171], v[112:127]\n"
      "332:\n"
      "s_waitcnt vmcnt(0) lgkmcnt(0)\n"
      "v_mfma_f32_32x32x16_bf16 v[0:15], v[128:131], v[156:159], v[0:15]\n"
      "v_xor_b32_sdwa v164, v184, v200 dst_sel:DWORD dst_unused:UNUSED_PAD src0_sel:WORD_1 src1_sel:DWORD\n"
      "ds_read_b128 v[164:167], v164 offset:61504\n"
      "v_xor_b32_sdwa v168, v198, v200 dst_sel:DWORD dst_unused:UNUSED_PAD src0_sel:WORD_1 src1_sel:DWORD\n"
      "v_mfma_f32_32x32x16_bf16 v[16:31], v[128:131], v[160:163], v[16:31]\n"
      "ds_read_b128 v[168:171], v168 offset:61504\n"
      "ds_read_b64_tr_b16 v[140:141], v185 offset:3072\n"
      "ds_read_b64_tr_b16 v[142:143], v185 offset:3840\n"
      "v_mfma_f32_32x32x16_bf16 v[32:47], v[132:135], v[156:159], v[32:47]\n"
      "ds_read_b64_tr_b16 v[144:145], v185 offset:3136\n"
      "ds_read_b64_tr_b16 v[146:147], v185 offset:3904\n"
      "ds_read_b64_tr_b16 v[148:149], v185 offset:3200\n"
      "ds_read_b64_tr_b16 v[150:151], v185 offset:3968\n"
      "v_mfma_f32_32x32x16_bf16 v[48:63], v[132:135], v[160:163], v[48:63]\n"
      "v_mfma_f32_32x32x16_bf16 v[64:79], v[136:139], v[156:159], v[64:79]\n"
      "v_mfma_f32_32x32x16_bf16 v[80:95], v[136:139], v[160:163], v[80:95]\n"
      "s_cmp_lg_u32 %[wv], 0\n"
      "s_cbranch_scc1 342f\n"
      "v_mfma_f32_32x32x16_bf16 v[96:111], v[152:155], v[156:159], v[96:111]\n"
      "v_mfma_f32_32x32x16_bf16 v[112:127], v[152:155], v[160:163], v[112:127]\n"
      "342:\n"
      "s_waitcnt vmcnt(0) lgkmcnt(0)\n"
      "v_mfma_f32_32x32x16_bf16 v[0:15], v[140:143], v[164:167], v[0:15]\n"
      "v_mfma_f32_32x32x16_bf16 v[16:31], v[140:143], v[168:171], v[16:31]\n"
      "v_mfma_f32_32x32x16_bf16 v[32:47], v[144:147], v[164:167], v[32:47]\n"
      "v_mfma_f32_32x32x16_bf16 v[48:63], v[144:147], v[168:171], v[48:63]\n"
      "v_mfma_f32_32x32x16_bf16 v[64:79], v[148:151], v[164:167], v[64:79]\n"
      "v_mfma_f32_32x32x16_bf16 v[80:95], v[148:151], v[168:171], v[80:95]\n"
      "s_cmp_lg_u32 %[wv], 1\n"
      "s_cbranch_scc1 352f\n"
      "v_mfma_f32_32x32x16_bf16 v[96:111], v[152:155], v[164:167], v[96:111]\n"
      "v_mfma_f32_32x32x16_bf16 v[112:127], v[152:155], v[168:171], v[112:127]\n"
      "352:\n"
      "s_setprio 0\n"
      "v_mbcnt_lo_u32_b32 v206, -1, 0\n"
      "v_mbcnt_hi_u32_b32 v206, -1, v206\n"
      "v_lshl_add_u32 v185, v206, 4, s59\n"
      "v_mov_b32 v144, 0xbdd2d3e8\n"
      "v_mov_b32 v145, 0\n"
      "v_mov_b32 v146, 0\n"
      "v_mov_b32 v147, 0\n"
      "v_mov_b32 v148, 0\n"
      "v_mov_b32 v199, 61376\n"
      "v_mov_b32 v202, 1\n"
      "s_nop 4\n"
      "ds_write_b128 v185, v[96:99] offset:0\n"
      "ds_write_b128 v185, v[100:103] offset:1024\n"
      "ds_write_b128 v185, v[104:107] offset:2048\n"
      "ds_write_b128 v185, v[108:111] offset:3072\n"
      "ds_write_b128 v185, v[112:115] offset:4096\n"
      "ds_write_b128 v185, v[116:119] offset:5120\n"
      "ds_write_b128 v185, v[120:123] offset:6144\n"
      "ds_write_b128 v185, v[124:127] offset:7168\n"
      "s_waitcnt lgkmcnt(0)\n"
      "s_mov_b64 exec, 1\n"
      "ds_add_u32 v199, v202\n"
      "s_mov_b64 exec, -1\n"
      "ds_read_b128 v[128:131], v201 offset:1664\n"
      "ds_read_b128 v[132:135], v201 offset:1696\n"
      "ds_read_b128 v[136:139], v201 offset:1728\n"
      "ds_read_b128 v[140:143], v201 offset:1760\n"
      "s_waitcnt lgkmcnt(3)\n"
      "v_mul_f32 v150, v0, v0\n"
      "v_mul_f32 v151, v1, v1\n"
      "v_fmaak_f32 v150, v150, v144, 0xc0135761\n"
      "v_fmaak_f32 v151, v151, v144, 0xc0135761\n"
      "v_mul_f32 v150, v150, v0\n"
      "v_mul_f32 v151, v151, v1\n"
      "v_exp_f32 v150, v150\n"
      "v_exp_f32 v151, v151\n"
      "v_fma_f32 v150, v150, v128, v128\n"
      "v_fma_f32 v151, v151, v129, v129\n"
      "v_rcp_f32 v150, v150\n"
      "v_rcp_f32 v151, v151\n"
      "v_fmac_f32 v145, v0, v150\n"
      "v_fmac_f32 v146, v1, v151\n"
      "v_mul_f32 v150, v2, v2\n"
      "v_mul_f32 v151, v3, v3\n"
      "v_fmaak_f32 v150, v150, v144, 0xc0135761\n"
      "v_fmaak_f32 v151, v151, v144, 0xc0135761\n"
      "v_mul_f32 v150, v150, v2\n"
      "v_mul_f32 v151, v151, v3\n"
      "v_exp_f32 v150, v150\n"
      "v_exp_f32 v151, v151\n"
      "v_fma_f32 v150, v150, v130, v130\n"
      "v_fma_f32 v151, v151, v131, v131\n"
      "v_rcp_f32 v150, v150\n"
      "v_rcp_f32 v151, v151\n"
      "v_fmac_f32 v145, v2, v150\n"
      "v_fmac_f32 v146, v3, v151\n"
      "v_mul_f32 v150, v16, v16\n"
      "v_mul_f32 v151, v17, v17\n"
      "v_fmaak_f32 v150, v150, v144, 0xc0135761\n"
      "v_fmaak_f32 v151, v151, v144, 0xc0135761\n"
      "v_mul_f32 v150, v150, v16\n"
      "v_mul_f32 v151, v151, v17\n"
      "v_exp_f32 v150, v150\n"
      "v_exp_f32 v151, v151\n"
      "v_fma_f32 v150, v150, v128, v128\n"
      "v_fma_f32 v151, v151, v129, v129\n"
      "v_rcp_f32 v150, v150\n"
      "v_rcp_f32 v151, v151\n"
      "v_fmac_f32 v147, v16, v150\n"
      "v_fmac_f32 v148, v17, v151\n"
      "v_mul_f32 v150, v18, v18\n"
      "v_mul_f32 v151, v19, v19\n"
      "v_fmaak_f32 v150, v150, v144, 0xc0135761\n"
      "v_fmaak_f32 v151, v151, v144, 0xc0135761\n"
      "v_mul_f32 v150, v150, v18\n"
      "v_mul_f32 v151, v151, v19\n"
      "v_exp_f32 v150, v150\n"
      "v_exp_f32 v151, v151\n"
      "v_fma_f32 v150, v150, v130, v130\n"
      "v_fma_f32 v151, v151, v131, v131\n"
      "v_rcp_f32 v150, v150\n"
      "v_rcp_f32 v151, v151\n"
      "v_fmac_f32 v147, v18, v150\n"
      "v_fmac_f32 v148, v19, v151\n"
      "ds_read_b128 v[128:131], v201 offset:1792\n"
      "s_waitcnt lgkmcnt(3)\n"
      "v_mul_f32 v150, v4, v4\n"
      "v_mul_f32 v151, v5, v5\n"
      "v_fmaak_f32 v150, v150, v144, 0xc0135761\n"
      "v_fmaak_f32 v151, v151, v144, 0xc0135761\n"
      "v_mul_f32 v150, v150, v4\n"
      "v_mul_f32 v151, v151, v5\n"
      "v_exp_f32 v150, v150\n"
      "v_exp_f32 v151, v151\n"
      "v_fma_f32 v150, v150, v132, v132\n"
      "v_fma_f32 v151, v151, v133, v133\n"
      "v_rcp_f32 v150, v150\n"
      "v_rcp_f32 v151, v151\n"
      "v_fmac_f32 v145, v4, v150\n"
      "v_fmac_f32 v146, v5, v151\n"
      "v_mul_f32 v150, v6, v6\n"
      "v_mul_f32 v151, v7, v7\n"
      "v_fmaak_f32 v150, v150, v144, 0xc0135761\n"
      "v_fmaak_f32 v151, v151, v144, 0xc0135761\n"
      "v_mul_f32 v150, v150, v6\n"
      "v_mul_f32 v151, v151, v7\n"
      "v_exp_f32 v150, v150\n"
      "v_exp_f32 v151, v151\n"
      "v_fma_f32 v150, v150, v134, v134\n"
      "v_fma_f32 v151, v151, v135, v135\n"
      "v_rcp_f32 v150, v150\n"
      "v_rcp_f32 v151, v151\n"
      "v_fmac_f32 v145, v6, v150\n"
      "v_fmac_f32 v146, v7, v151\n"
      "v_mul_f32 v150, v20, v20\n"
      "v_mul_f32 v151, v21, v21\n"
      "v_fmaak_f32 v150, v150, v144, 0xc0135761\n"
      "v_fmaak_f32 v151, v151, v144, 0xc0135761\n"
      "v_mul_f32 v150, v150, v20\n"
      "v_mul_f32 v151, v151, v21\n"
      "v_exp_f32 v150, v150\n"
      "v_exp_f32 v151, v151\n"
      "v_fma_f32 v150, v150, v132, v132\n"
      "v_fma_f32 v151, v151, v133, v133\n"
      "v_rcp_f32 v150, v150\n"
      "v_rcp_f32 v151, v151\n"
      "v_fmac_f32 v147, v20, v150\n"
      "v_fmac_f32 v148, v21, v151\n"
      "v_mul_f32 v150, v22, v22\n"
      "v_mul_f32 v151, v23, v23\n"
      "v_fmaak_f32 v150, v150, v144, 0xc0135761\n"
      "v_fmaak_f32 v151, v151, v144, 0xc0135761\n"
      "v_mul_f32 v150, v150, v22\n"
      "v_mul_f32 v151, v151, v23\n"
      "v_exp_f32 v150, v150\n"
      "v_exp_f32 v151, v151\n"
      "v_fma_f32 v150, v150, v134, v134\n"
      "v_fma_f32 v151, v151, v135, v135\n"
      "v_rcp_f32 v150, v150\n"
      "v_rcp_f32 v151, v151\n"
      "v_fmac_f32 v147, v22, v150\n"
      "v_fmac_f32 v148, v23, v151\n"
      "ds_read_b128 v[132:135], v201 offset:1824\n"
      "s_waitcnt lgkmcnt(3)\n"
      "v_mul_f32 v150, v8, v8\n"
      "v_mul_f32 v151, v9, v9\n"
      "v_fmaak_f32 v150, v150, v144, 0xc0135761\n"
      "v_fmaak_f32 v151, v151, v144, 0xc0135761\n"
      "v_mul_f32 v150, v150, v8\n"
      "v_mul_f32 v151, v151, v9\n"
      "v_exp_f32 v150, v150\n"
      "v_exp_f32 v151, v151\n"
      "v_fma_f32 v150, v150, v136, v136\n"
      "v_fma_f32 v151, v151, v137, v137\n"
      "v_rcp_f32 v150, v150\n"
      "v_rcp_f32 v151, v151\n"
      "v_fmac_f32 v145, v8, v150\n"
      "v_fmac_f32 v146, v9, v151\n"
      "v_mul_f32 v150, v10, v10\n"
      "v_mul_f32 v151, v11, v11\n"
      "v_fmaak_f32 v150, v150, v144, 0xc0135761\n"
      "v_fmaak_f32 v151, v151, v144, 0xc0135761\n"
      "v_mul_f32 v150, v150, v10\n"
      "v_mul_f32 v151, v151, v11\n"
      "v_exp_f32 v150, v150\n"
      "v_exp_f32 v151, v151\n"
      "v_fma_f32 v150, v150, v138, v138\n"
      "v_fma_f32 v151, v151, v139, v139\n"
      "v_rcp_f32 v150, v150\n"
      "v_rcp_f32 v151, v151\n"
      "v_fmac_f32 v145, v10, v150\n"
      "v_fmac_f32 v146, v11, v151\n"
      "v_mul_f32 v150, v24, v24\n"
      "v_mul_f32 v151, v25, v25\n"
      "v_fmaak_f32 v150, v150, v144, 0xc0135761\n"
      "v_fmaak_f32 v151, v151, v144, 0xc0135761\n"
      "v_mul_f32 v150, v150, v24\n"
      "v_mul_f32 v151, v151, v25\n"
      "v_exp_f32 v150, v150\n"
      "v_exp_f32 v151, v151\n"
      "v_fma_f32 v150, v150, v136, v136\n"
      "v_fma_f32 v151, v151, v137, v137\n"
      "v_rcp_f32 v150, v150\n"
      "v_rcp_f32 v151, v151\n"
      "v_fmac_f32 v147, v24, v150\n"
      "v_fmac_f32 v148, v25, v151\n"
      "v_mul_f32 v150, v26, v26\n"
      "v_mul_f32 v151, v27, v27\n"
      "v_fmaak_f32 v150, v150, v144, 0xc0135761\n"
      "v_fmaak_f32 v151, v151, v144, 0xc0135761\n"
      "v_mul_f32 v150, v150, v26\n"
      "v_mul_f32 v151, v151, v27\n"
      "v_exp_f32 v150, v150\n"
      "v_exp_f32 v151, v151\n"
      "v_fma_f32 v150, v150, v138, v138\n"
      "v_fma_f32 v151, v151, v139, v139\n"
      "v_rcp_f32 v150, v150\n"
      "v_rcp_f32 v151, v151\n"
      "v_fmac_f32 v147, v26, v150\n"
      "v_fmac_f32 v148, v27, v151\n"
      "ds_read_b128 v[136:139], v201 offset:1856\n"
      "s_waitcnt lgkmcnt(3)\n"
      "v_mul_f32 v150, v12, v12\n"
      "v_mul_f32 v151, v13, v13\n"
      "v_fmaak_f32 v150, v150, v144, 0xc0135761\n"
      "v_fmaak_f32 v151, v151, v144, 0xc0135761\n"
      "v_mul_f32 v150, v150, v12\n"
      "v_mul_f32 v151, v151, v13\n"
      "v_exp_f32 v150, v150\n"
      "v_exp_f32 v151, v151\n"
      "v_fma_f32 v150, v150, v140, v140\n"
      "v_fma_f32 v151, v151, v141, v141\n"
      "v_rcp_f32 v150, v150\n"
      "v_rcp_f32 v151, v151\n"
      "v_fmac_f32 v145, v12, v150\n"
      "v_fmac_f32 v146, v13, v151\n"
      "v_mul_f32 v150, v14, v14\n"
      "v_mul_f32 v151, v15, v15\n"
      "v_fmaak_f32 v150, v150, v144, 0xc0135761\n"
      "v_fmaak_f32 v151, v151, v144, 0xc0135761\n"
      "v_mul_f32 v150, v150, v14\n"
      "v_mul_f32 v151, v151, v15\n"
      "v_exp_f32 v150, v150\n"
      "v_exp_f32 v151, v151\n"
      "v_fma_f32 v150, v150, v142, v142\n"
      "v_fma_f32 v151, v151, v143, v143\n"
      "v_rcp_f32 v150, v150\n"
      "v_rcp_f32 v151, v151\n"
      "v_fmac_f32 v145, v14, v150\n"
      "v_fmac_f32 v146, v15, v151\n"
      "v_mul_f32 v150, v28, v28\n"
      "v_mul_f32 v151, v29, v29\n"
      "v_fmaak_f32 v150, v150, v144, 0xc0135761\n"
      "v_fmaak_f32 v151, v151, v144, 0xc0135761\n"
      "v_mul_f32 v150, v150, v28\n"
      "v_mul_f32 v151, v151, v29\n"
      "v_exp_f32 v150, v150\n"
      "v_exp_f32 v151, v151\n"
      "v_fma_f32 v150, v150, v140, v140\n"
      "v_fma_f32 v151, v151, v141, v141\n"
      "v_rcp_f32 v150, v150\n"
      "v_rcp_f32 v151, v151\n"
      "v_fmac_f32 v147, v28, v150\n"
      "v_fmac_f32 v148, v29, v151\n"
      "v_mul_f32 v150, v30, v30\n"
      "v_mul_f32 v151, v31, v31\n"
      "v_fmaak_f32 v150, v150, v144, 0xc0135761\n"
      "v_fmaak_f32 v151, v151, v144, 0xc0135761\n"
      "v_mul_f32 v150, v150, v30\n"
      "v_mul_f32 v151, v151, v31\n"
      "v_exp_f32 v150, v150\n"
      "v_exp_f32 v151, v151\n"
      "v_fma_f32 v150, v150, v142, v142\n"
      "v_fma_f32 v151, v151, v143, v143\n"
      "v_rcp_f32 v150, v150\n"
      "v_rcp_f32 v151, v151\n"
      "v_fmac_f32 v147, v30, v150\n"
      "v_fmac_f32 v148, v31, v151\n"
      "ds_read_b128 v[140:143], v201 offset:1888\n"
      "s_waitcnt lgkmcnt(3)\n"
      "v_mul_f32 v150, v32, v32\n"
      "v_mul_f32 v151, v33, v33\n"
      "v_fmaak_f32 v150, v150, v144, 0xc0135761\n"
      "v_fmaak_f32 v151, v151, v144, 0xc0135761\n"
      "v_mul_f32 v150, v150, v32\n"
      "v_mul_f32 v151, v151, v33\n"
      "v_exp_f32 v150, v150\n"
      "v_exp_f32 v151, v151\n"
      "v_fma_f32 v150, v150, v128, v128\n"
      "v_fma_f32 v151, v151, v129, v129\n"
      "v_rcp_f32 v150, v150\n"
      "v_rcp_f32 v151, v151\n"
      "v_fmac_f32 v145, v32, v150\n"
      "v_fmac_f32 v146, v33, v151\n"
      "v_mul_f32 v150, v34, v34\n"
      "v_mul_f32 v151, v35, v35\n"
      "v_fmaak_f32 v150, v150, v144, 0xc0135761\n"
      "v_fmaak_f32 v151, v151, v144, 0xc0135761\n"
      "v_mul_f32 v150, v150, v34\n"
      "v_mul_f32 v151, v151, v35\n"
      "v_exp_f32 v150, v150\n"
      "v_exp_f32 v151, v151\n"
      "v_fma_f32 v150, v150, v130, v130\n"
      "v_fma_f32 v151, v151, v131, v131\n"
      "v_rcp_f32 v150, v150\n"
      "v_rcp_f32 v151, v151\n"
      "v_fmac_f32 v145, v34, v150\n"
      "v_fmac_f32 v146, v35, v151\n"
      "v_mul_f32 v150, v48, v48\n"
      "v_mul_f32 v151, v49, v49\n"
      "v_fmaak_f32 v150, v150, v144, 0xc0135761\n"
      "v_fmaak_f32 v151, v151, v144, 0xc0135761\n"
      "v_mul_f32 v150, v150, v48\n"
      "v_mul_f32 v151, v151, v49\n"
      "v_exp_f32 v150, v150\n"
      "v_exp_f32 v151, v151\n"
      "v_fma_f32 v150, v150, v128, v128\n"
      "v_fma_f32 v151, v151, v129, v129\n"
      "v_rcp_f32 v150, v150\n"
      "v_rcp_f32 v151, v151\n"
      "v_fmac_f32 v147, v48, v150\n"
      "v_fmac_f32 v148, v49, v151\n"
      "v_mul_f32 v150, v50, v50\n"
      "v_mul_f32 v151, v51, v51\n"
      "v_fmaak_f32 v150, v150, v144, 0xc0135761\n"
      "v_fmaak_f32 v151, v151, v144, 0xc0135761\n"
      "v_mul_f32 v150, v150, v50\n"
      "v_mul_f32 v151, v151, v51\n"
      "v_exp_f32 v150, v150\n"
      "v_exp_f32 v151, v151\n"
      "v_fma_f32 v150, v150, v130, v130\n"
      "v_fma_f32 v151, v151, v131, v131\n"
      "v_rcp_f32 v150, v150\n"
      "v_rcp_f32 v151, v151\n"
      "v_fmac_f32 v147, v50, v150\n"
      "v_fmac_f32 v148, v51, v151\n"
      "ds_read_b128 v[128:131], v201 offset:1920\n"
      "s_waitcnt lgkmcnt(3)\n"
      "v_mul_f32 v150, v36, v36\n"
      "v_mul_f32 v151, v37, v37\n"
      "v_fmaak_f32 v150, v150, v144, 0xc0135761\n"
      "v_fmaak_f32 v151, v151, v144, 0xc0135761\n"
      "v_mul_f32 v150, v150, v36\n"
      "v_mul_f32 v151, v151, v37\n"
      "v_exp_f32 v150, v150\n"
      "v_exp_f32 v151, v151\n"
      "v_fma_f32 v150, v150, v132, v132\n"
      "v_fma_f32 v151, v151, v133, v133\n"
      "v_rcp_f32 v150, v150\n"
      "v_rcp_f32 v151, v151\n"
      "v_fmac_f32 v145, v36, v150\n"
      "v_fmac_f32 v146, v37, v151\n"
      "v_mul_f32 v150, v38, v38\n"
      "v_mul_f32 v151, v39, v39\n"
      "v_fmaak_f32 v150, v150, v144, 0xc0135761\n"
      "v_fmaak_f32 v151, v151, v144, 0xc0135761\n"
      "v_mul_f32 v150, v150, v38\n"
      "v_mul_f32 v151, v151, v39\n"
      "v_exp_f32 v150, v150\n"
      "v_exp_f32 v151, v151\n"
      "v_fma_f32 v150, v150, v134, v134\n"
      "v_fma_f32 v151, v151, v135, v135\n"
      "v_rcp_f32 v150, v150\n"
      "v_rcp_f32 v151, v151\n"
      "v_fmac_f32 v145, v38, v150\n"
      "v_fmac_f32 v146, v39, v151\n"
      "v_mul_f32 v150, v52, v52\n"
      "v_mul_f32 v151, v53, v53\n"
      "v_fmaak_f32 v150, v150, v144, 0xc0135761\n"
      "v_fmaak_f32 v151, v151, v144, 0xc0135761\n"
      "v_mul_f32 v150, v150, v52\n"
      "v_mul_f32 v151, v151, v53\n"
      "v_exp_f32 v150, v150\n"
      "v_exp_f32 v151, v151\n"
      "v_fma_f32 v150, v150, v132, v132\n"
      "v_fma_f32 v151, v151, v133, v133\n"
      "v_rcp_f32 v150, v150\n"
      "v_rcp_f32 v151, v151\n"
      "v_fmac_f32 v147, v52, v150\n"
      "v_fmac_f32 v148, v53, v151\n"
      "v_mul_f32 v150, v54, v54\n"
      "v_mul_f32 v151, v55, v55\n"
      "v_fmaak_f32 v150, v150, v144, 0xc0135761\n"
      "v_fmaak_f32 v151, v151, v144, 0xc0135761\n"
      "v_mul_f32 v150, v150, v54\n"
      "v_mul_f32 v151, v151, v55\n"
      "v_exp_f32 v150, v150\n"
      "v_exp_f32 v151, v151\n"
      "v_fma_f32 v150, v150, v134, v134\n"
      "v_fma_f32 v151, v151, v135, v135\n"
      "v_rcp_f32 v150, v150\n"
      "v_rcp_f32 v151, v151\n"
      "v_fmac_f32 v147, v54, v150\n"
      "v_fmac_f32 v148, v55, v151\n"
      "ds_read_b128 v[132:135], v201 offset:1952\n"
      "s_waitcnt lgkmcnt(3)\n"
      "v_mul_f32 v150, v40, v40\n"
      "v_mul_f32 v151, v41, v41\n"
      "v_fmaak_f32 v150, v150, v144, 0xc0135761\n"
      "v_fmaak_f32 v151, v151, v144, 0xc0135761\n"
      "v_mul_f32 v150, v150, v40\n"
      "v_mul_f32 v151, v151, v41\n"
      "v_exp_f32 v150, v150\n"
      "v_exp_f32 v151, v151\n"
      "v_fma_f32 v150, v150, v136, v136\n"
      "v_fma_f32 v151, v151, v137, v137\n"
      "v_rcp_f32 v150, v150\n"
      "v_rcp_f32 v151, v151\n"
      "v_fmac_f32 v145, v40, v150\n"
      "v_fmac_f32 v146, v41, v151\n"
      "v_mul_f32 v150, v42, v42\n"
      "v_mul_f32 v151, v43, v43\n"
      "v_fmaak_f32 v150, v150, v144, 0xc0135761\n"
      "v_fmaak_f32 v151, v151, v144, 0xc0135761\n"
      "v_mul_f32 v150, v150, v42\n"
      "v_mul_f32 v151, v151, v43\n"
      "v_exp_f32 v150, v150\n"
      "v_exp_f32 v151, v151\n"
      "v_fma_f32 v150, v150, v138, v138\n"
      "v_fma_f32 v151, v151, v139, v139\n"
      "v_rcp_f32 v150, v150\n"
      "v_rcp_f32 v151, v151\n"
      "v_fmac_f32 v145, v42, v150\n"
      "v_fmac_f32 v146, v43, v151\n"
      "v_mul_f32 v150, v56, v56\n"
      "v_mul_f32 v151, v57, v57\n"
      "v_fmaak_f32 v150, v150, v144, 0xc0135761\n"
      "v_fmaak_f32 v151, v151, v144, 0xc0135761\n"
      "v_mul_f32 v150, v150, v56\n"
      "v_mul_f32 v151, v151, v57\n"
      "v_exp_f32 v150, v150\n"
      "v_exp_f32 v151, v151\n"
      "v_fma_f32 v150, v150, v136, v136\n"
      "v_fma_f32 v151, v151, v137, v137\n"
      "v_rcp_f32 v150, v150\n"
      "v_rcp_f32 v151, v151\n"
      "v_fmac_f32 v147, v56, v150\n"
      "v_fmac_f32 v148, v57, v151\n"
      "v_mul_f32 v150, v58, v58\n"
      "v_mul_f32 v151, v59, v59\n"
      "v_fmaak_f32 v150, v150, v144, 0xc0135761\n"
      "v_fmaak_f32 v151, v151, v144, 0xc0135761\n"
      "v_mul_f32 v150, v150, v58\n"
      "v_mul_f32 v151, v151, v59\n"
      "v_exp_f32 v150, v150\n"
      "v_exp_f32 v151, v151\n"
      "v_fma_f32 v150, v150, v138, v138\n"
      "v_fma_f32 v151, v151, v139, v139\n"
      "v_rcp_f32 v150, v150\n"
      "v_rcp_f32 v151, v151\n"
      "v_fmac_f32 v147, v58, v150\n"
      "v_fmac_f32 v148, v59, v151\n"
      "ds_read_b128 v[136:139], v201 offset:1984\n"
      "s_waitcnt lgkmcnt(3)\n"
      "v_mul_f32 v150, v44, v44\n"
      "v_mul_f32 v151, v45, v45\n"
      "v_fmaak_f32 v150, v150, v144, 0xc0135761\n"
      "v_fmaak_f32 v151, v151, v144, 0xc0135761\n"
      "v_mul_f32 v150, v150, v44\n"
      "v_mul_f32 v151, v151, v45\n"
      "v_exp_f32 v150, v150\n"
      "v_exp_f32 v151, v151\n"
      "v_fma_f32 v150, v150, v140, v140\n"
      "v_fma_f32 v151, v151, v141, v141\n"
      "v_rcp_f32 v150, v150\n"
      "v_rcp_f32 v151, v151\n"
      "v_fmac_f32 v145, v44, v150\n"
      "v_fmac_f32 v146, v45, v151\n"
      "v_mul_f32 v150, v46, v46\n"
      "v_mul_f32 v151, v47, v47\n"
      "v_fmaak_f32 v150, v150, v144, 0xc0135761\n"
      "v_fmaak_f32 v151, v151, v144, 0xc0135761\n"
      "v_mul_f32 v150, v150, v46\n"
      "v_mul_f32 v151, v151, v47\n"
      "v_exp_f32 v150, v150\n"
      "v_exp_f32 v151, v151\n"
      "v_fma_f32 v150, v150, v142, v142\n"
      "v_fma_f32 v151, v151, v143, v143\n"
      "v_rcp_f32 v150, v150\n"
      "v_rcp_f32 v151, v151\n"
      "v_fmac_f32 v145, v46, v150\n"
      "v_fmac_f32 v146, v47, v151\n"
      "v_mul_f32 v150, v60, v60\n"
      "v_mul_f32 v151, v61, v61\n"
      "v_fmaak_f32 v150, v150, v144, 0xc0135761\n"
      "v_fmaak_f32 v151, v151, v144, 0xc0135761\n"
      "v_mul_f32 v150, v150, v60\n"
      "v_mul_f32 v151, v151, v61\n"
      "v_exp_f32 v150, v150\n"
      "v_exp_f32 v151, v151\n"
      "v_fma_f32 v150, v150, v140, v140\n"
      "v_fma_f32 v151, v151, v141, v141\n"
      "v_rcp_f32 v150, v150\n"
      "v_rcp_f32 v151, v151\n"
      "v_fmac_f32 v147, v60, v150\n"
      "v_fmac_f32 v148, v61, v151\n"
      "v_mul_f32 v150, v62, v62\n"
      "v_mul_f32 v151, v63, v63\n"
      "v_fmaak_f32 v150, v150, v144, 0xc0135761\n"
      "v_fmaak_f32 v151, v151, v144, 0xc0135761\n"
      "v_mul_f32 v150, v150, v62\n"
      "v_mul_f32 v151, v151, v63\n"
      "v_exp_f32 v150, v150\n"
      "v_exp_f32 v151, v151\n"
      "v_fma_f32 v150, v150, v142, v142\n"
      "v_fma_f32 v151, v151, v143, v143\n"
      "v_rcp_f32 v150, v150\n"
      "v_rcp_f32 v151, v151\n"
      "v_fmac_f32 v147, v62, v150\n"
      "v_fmac_f32 v148, v63, v151\n"
      "ds_read_b128 v[140:143], v201 offset:2016\n"
      "s_waitcnt lgkmcnt(3)\n"
      "v_mul_f32 v150, v64, v64\n"
      "v_mul_f32 v151, v65, v65\n"
      "v_fmaak_f32 v150, v150, v144, 0xc0135761\n"
      "v_fmaak_f32 v151, v151, v144, 0xc0135761\n"
      "v_mul_f32 v150, v150, v64\n"
      "v_mul_f32 v151, v151, v65\n"
      "v_exp_f32 v150, v150\n"
      "v_exp_f32 v151, v151\n"
      "v_fma_f32 v150, v150, v128, v128\n"
      "v_fma_f32 v151, v151, v129, v129\n"
      "v_rcp_f32 v150, v150\n"
      "v_rcp_f32 v151, v151\n"
      "v_fmac_f32 v145, v64, v150\n"
      "v_fmac_f32 v146, v65, v151\n"
      "v_mul_f32 v150, v66, v66\n"
      "v_mul_f32 v151, v67, v67\n"
      "v_fmaak_f32 v150, v150, v144, 0xc0135761\n"
      "v_fmaak_f32 v151, v151, v144, 0xc0135761\n"
      "v_mul_f32 v150, v150, v66\n"
      "v_mul_f32 v151, v151, v67\n"
      "v_exp_f32 v150, v150\n"
      "v_exp_f32 v151, v151\n"
      "v_fma_f32 v150, v150, v130, v130\n"
      "v_fma_f32 v151, v151, v131, v131\n"
      "v_rcp_f32 v150, v150\n"
      "v_rcp_f32 v151, v151\n"
      "v_fmac_f32 v145, v66, v150\n"
      "v_fmac_f32 v146, v67, v151\n"
      "v_mul_f32 v150, v80, v80\n"
      "v_mul_f32 v151, v81, v81\n"
      "v_fmaak_f32 v150, v150, v144, 0xc0135761\n"
      "v_fmaak_f32 v151, v151, v144, 0xc0135761\n"
      "v_mul_f32 v150, v150, v80\n"
      "v_mul_f32 v151, v151, v81\n"
      "v_exp_f32 v150, v150\n"
      "v_exp_f32 v151, v151\n"
      "v_fma_f32 v150, v150, v128, v128\n"
      "v_fma_f32 v151, v151, v129, v129\n"
      "v_rcp_f32 v150, v150\n"
      "v_rcp_f32 v151, v151\n"
      "v_fmac_f32 v147, v80, v150\n"
      "v_fmac_f32 v148, v81, v151\n"
      "v_mul_f32 v150, v82, v82\n"
      "v_mul_f32 v151, v83, v83\n"
      "v_fmaak_f32 v150, v150, v144, 0xc0135761\n"
      "v_fmaak_f32 v151, v151, v144, 0xc0135761\n"
      "v_mul_f32 v150, v150, v82\n"
      "v_mul_f32 v151, v151, v83\n"
      "v_exp_f32 v150, v150\n"
      "v_exp_f32 v151, v151\n"
      "v_fma_f32 v150, v150, v130, v130\n"
      "v_fma_f32 v151, v151, v131, v131\n"
      "v_rcp_f32 v150, v150\n"
      "v_rcp_f32 v151, v151\n"
      "v_fmac_f32 v147, v82, v150\n"
      "v_fmac_f32 v148, v83, v151\n"
      "s_waitcnt lgkmcnt(2)\n"
      "v_mul_f32 v150, v68, v68\n"
      "v_mul_f32 v151, v69, v69\n"
      "v_fmaak_f32 v150, v150, v144, 0xc0135761\n"
      "v_fmaak_f32 v151, v151, v144, 0xc0135761\n"
      "v_mul_f32 v150, v150, v68\n"
      "v_mul_f32 v151, v151, v69\n"
      "v_exp_f32 v150, v150\n"
      "v_exp_f32 v151, v151\n"
      "v_fma_f32 v150, v150, v132, v132\n"
      "v_fma_f32 v151, v151, v133, v133\n"
      "v_rcp_f32 v150, v150\n"
      "v_rcp_f32 v151, v151\n"
      "v_fmac_f32 v145, v68, v150\n"
      "v_fmac_f32 v146, v69, v151\n"
      "v_mul_f32 v150, v70, v70\n"
      "v_mul_f32 v151, v71, v71\n"
      "v_fmaak_f32 v150, v150, v144, 0xc0135761\n"
      "v_fmaak_f32 v151, v151, v144, 0xc0135761\n"
      "v_mul_f32 v150, v150, v70\n"
      "v_mul_f32 v151, v151, v71\n"
      "v_exp_f32 v150, v150\n"
      "v_exp_f32 v151, v151\n"
      "v_fma_f32 v150, v150, v134, v134\n"
      "v_fma_f32 v151, v151, v135, v135\n"
      "v_rcp_f32 v150, v150\n"
      "v_rcp_f32 v151, v151\n"
      "v_fmac_f32 v145, v70, v150\n"
      "v_fmac_f32 v146, v71, v151\n"
      "v_mul_f32 v150, v84, v84\n"
      "v_mul_f32 v151, v85, v85\n"
      "v_fmaak_f32 v150, v150, v144, 0xc0135761\n"
      "v_fmaak_f32 v151, v151, v144, 0xc0135761\n"
      "v_mul_f32 v150, v150, v84\n"
      "v_mul_f32 v151, v151, v85\n"
      "v_exp_f32 v150, v150\n"
      "v_exp_f32 v151, v151\n"
      "v_fma_f32 v150, v150, v132, v132\n"
      "v_fma_f32 v151, v151, v133, v133\n"
      "v_rcp_f32 v150, v150\n"
      "v_rcp_f32 v151, v151\n"
      "v_fmac_f32 v147, v84, v150\n"
      "v_fmac_f32 v148, v85, v151\n"
      "v_mul_f32 v150, v86, v86\n"
      "v_mul_f32 v151, v87, v87\n"
      "v_fmaak_f32 v150, v150, v144, 0xc0135761\n"
      "v_fmaak_f32 v151, v151, v144, 0xc0135761\n"
      "v_mul_f32 v150, v150, v86\n"
      "v_mul_f32 v151, v151, v87\n"
      "v_exp_f32 v150, v150\n"
      "v_exp_f32 v151, v151\n"
      "v_fma_f32 v150, v150, v134, v134\n"
      "v_fma_f32 v151, v151, v135, v135\n"
      "v_rcp_f32 v150, v150\n"
      "v_rcp_f32 v151, v151\n"
      "v_fmac_f32 v147, v86, v150\n"
      "v_fmac_f32 v148, v87, v151\n"
      "s_waitcnt lgkmcnt(1)\n"
      "v_mul_f32 v150, v72, v72\n"
      "v_mul_f32 v151, v73, v73\n"
      "v_fmaak_f32 v150, v150, v144, 0xc0135761\n"
      "v_fmaak_f32 v151, v151, v144, 0xc0135761\n"
      "v_mul_f32 v150, v150, v72\n"
      "v_mul_f32 v151, v151, v73\n"
      "v_exp_f32 v150, v150\n"
      "v_exp_f32 v151, v151\n"
      "v_fma_f32 v150, v150, v136, v136\n"
      "v_fma_f32 v151, v151, v137, v137\n"
      "v_rcp_f32 v150, v150\n"
      "v_rcp_f32 v151, v151\n"
      "v_fmac_f32 v145, v72, v150\n"
      "v_fmac_f32 v146, v73, v151\n"
      "v_mul_f32 v150, v74, v74\n"
      "v_mul_f32 v151, v75, v75\n"
      "v_fmaak_f32 v150, v150, v144, 0xc0135761\n"
      "v_fmaak_f32 v151, v151, v144, 0xc0135761\n"
      "v_mul_f32 v150, v150, v74\n"
      "v_mul_f32 v151, v151, v75\n"
      "v_exp_f32 v150, v150\n"
      "v_exp_f32 v151, v151\n"
      "v_fma_f32 v150, v150, v138, v138\n"
      "v_fma_f32 v151, v151, v139, v139\n"
      "v_rcp_f32 v150, v150\n"
      "v_rcp_f32 v151, v151\n"
      "v_fmac_f32 v145, v74, v150\n"
      "v_fmac_f32 v146, v75, v151\n"
      "v_mul_f32 v150, v88, v88\n"
      "v_mul_f32 v151, v89, v89\n"
      "v_fmaak_f32 v150, v150, v144, 0xc0135761\n"
      "v_fmaak_f32 v151, v151, v144, 0xc0135761\n"
      "v_mul_f32 v150, v150, v88\n"
      "v_mul_f32 v151, v151, v89\n"
      "v_exp_f32 v150, v150\n"
      "v_exp_f32 v151, v151\n"
      "v_fma_f32 v150, v150, v136, v136\n"
      "v_fma_f32 v151, v151, v137, v137\n"
      "v_rcp_f32 v150, v150\n"
      "v_rcp_f32 v151, v151\n"
      "v_fmac_f32 v147, v88, v150\n"
      "v_fmac_f32 v148, v89, v151\n"
      "v_mul_f32 v150, v90, v90\n"
      "v_mul_f32 v151, v91, v91\n"
      "v_fmaak_f32 v150, v150, v144, 0xc0135761\n"
      "v_fmaak_f32 v151, v151, v144, 0xc0135761\n"
      "v_mul_f32 v150, v150, v90\n"
      "v_mul_f32 v151, v151, v91\n"
      "v_exp_f32 v150, v150\n"
      "v_exp_f32 v151, v151\n"
      "v_fma_f32 v150, v150, v138, v138\n"
      "v_fma_f32 v151, v151, v139, v139\n"
      "v_rcp_f32 v150, v150\n"
      "v_rcp_f32 v151, v151\n"
      "v_fmac_f32 v147, v90, v150\n"
      "v_fmac_f32 v148, v91, v151\n"
      "s_waitcnt lgkmcnt(0)\n"
      "v_mul_f32 v150, v76, v76\n"
      "v_mul_f32 v151, v77, v77\n"
      "v_fmaak_f32 v150, v150, v144, 0xc0135761\n"
      "v_fmaak_f32 v151, v151, v144, 0xc0135761\n"
      "v_mul_f32 v150, v150, v76\n"
      "v_mul_f32 v151, v151, v77\n"
      "v_exp_f32 v150, v150\n"
      "v_exp_f32 v151, v151\n"
      "v_fma_f32 v150, v150, v140, v140\n"
      "v_fma_f32 v151, v151, v141, v141\n"
      "v_rcp_f32 v150, v150\n"
      "v_rcp_f32 v151, v151\n"
      "v_fmac_f32 v145, v76, v150\n"
      "v_fmac_f32 v146, v77, v151\n"
      "v_mul_f32 v150, v78, v78\n"
      "v_mul_f32 v151, v79, v79\n"
      "v_fmaak_f32 v150, v150, v144, 0xc0135761\n"
      "v_fmaak_f32 v151, v151, v144, 0xc0135761\n"
      "v_mul_f32 v150, v150, v78\n"
      "v_mul_f32 v151, v151, v79\n"
      "v_exp_f32 v150, v150\n"
      "v_exp_f32 v151, v151\n"
      "v_fma_f32 v150, v150, v142, v142\n"
      "v_fma_f32 v151, v151, v143, v143\n"
      "v_rcp_f32 v150, v150\n"
      "v_rcp_f32 v151, v151\n"
      "v_fmac_f32 v145, v78, v150\n"
      "v_fmac_f32 v146, v79, v151\n"
      "v_mul_f32 v150, v92, v92\n"
      "v_mul_f32 v151, v93, v93\n"
      "v_fmaak_f32 v150, v150, v144, 0xc0135761\n"
      "v_fmaak_f32 v151, v151, v144, 0xc0135761\n"
      "v_mul_f32 v150, v150, v92\n"
      "v_mul_f32 v151, v151, v93\n"
      "v_exp_f32 v150, v150\n"
      "v_exp_f32 v151, v151\n"
      "v_fma_f32 v150, v150, v140, v140\n"
      "v_fma_f32 v151, v151, v141, v141\n"
      "v_rcp_f32 v150, v150\n"
      "v_rcp_f32 v151, v151\n"
      "v_fmac_f32 v147, v92, v150\n"
      "v_fmac_f32 v148, v93, v151\n"
      "v_mul_f32 v150, v94, v94\n"
      "v_mul_f32 v151, v95, v95\n"
      "v_fmaak_f32 v150, v150, v144, 0xc0135761\n"
      "v_fmaak_f32 v151, v151, v144, 0xc0135761\n"
      "v_mul_f32 v150, v150, v94\n"
      "v_mul_f32 v151, v151, v95\n"
      "v_exp_f32 v150, v150\n"
      "v_exp_f32 v151, v151\n"
      "v_fma_f32 v150, v150, v142, v142\n"
      "v_fma_f32 v151, v151, v143, v143\n"
      "v_rcp_f32 v150, v150\n"
      "v_rcp_f32 v151, v151\n"
      "v_fmac_f32 v147, v94, v150\n"
      "v_fmac_f32 v148, v95, v151\n"
      "7:\n"
      "ds_read_b32 v202, v199\n"
      "s_waitcnt lgkmcnt(0)\n"
      "v_readfirstlane_b32 s57, v202\n"
      "s_cmp_lt_u32 s57, 4\n"
      "s_cbranch_scc1 7b\n"
      "s_lshl_b32 s57, %[wv], 11\n"
      "s_add_u32 s57, s57, 106304\n"
      "v_lshl_add_u32 v185, v206, 4, s57\n"
      "s_and_b32 s57, %[wv], 1\n"
      "s_lshl_b32 s57, s57, 6\n"
      "s_add_u32 s57, s57, 96832\n"
      "v_add_u32 v201, s57, v200\n"
      "ds_read_b128 v[0:3], v185 offset:0\n"
      "ds_read_b128 v[4:7], v185 offset:1024\n"
      "ds_read_b128 v[8:11], v185 offset:14336\n"
      "ds_read_b128 v[12:15], v185 offset:15360\n"
      "ds_read_b128 v[16:19], v185 offset:28672\n"
      "ds_read_b128 v[20:23], v185 offset:29696\n"
      "ds_read_b128 v[24:27], v185 offset:43008\n"
      "ds_read_b128 v[28:31], v185 offset:44032\n"
      "ds_read_b128 v[32:35], v201 offset:1536\n"
      "ds_read_b128 v[36:39], v201 offset:1568\n"
      "v_mov_b32 v40, 0\n"
      "v_mov_b32 v41, 0\n"
      "s_waitcnt lgkmcnt(0)\n"
      "v_add_f32 v0, v0, v8\n"
      "v_add_f32 v16, v16, v24\n"
      "v_add_f32 v1, v1, v9\n"
      "v_add_f32 v17, v17, v25\n"
      "v_add_f32 v2, v2, v10\n"
      "v_add_f32 v18, v18, v26\n"
      "v_add_f32 v3, v3, v11\n"
      "v_add_f32 v19, v19, v27\n"
      "v_add_f32 v4, v4, v12\n"
      "v_add_f32 v20, v20, v28\n"
      "v_add_f32 v5, v5, v13\n"
      "v_add_f32 v21, v21, v29\n"
      "v_add_f32 v6, v6, v14\n"
      "v_add_f32 v22, v22, v30\n"
      "v_add_f32 v7, v7, v15\n"
      "v_add_f32 v23, v23, v31\n"
      "v_add_f32 v0, v0, v32\n"
      "v_add_f32 v1, v1, v33\n"
      "v_add_f32 v2, v2, v34\n"
      "v_add_f32 v3, v3, v35\n"
      "v_add_f32 v4, v4, v36\n"
      "v_add_f32 v5, v5, v37\n"
      "v_add_f32 v6, v6, v38\n"
      "v_add_f32 v7, v7, v39\n"
      "v_add_f32 v0, v0, v16\n"
      "v_add_f32 v1, v1, v17\n"
      "v_add_f32 v2, v2, v18\n"
      "v_add_f32 v3, v3, v19\n"
      "v_add_f32 v4, v4, v20\n"
      "v_add_f32 v5, v5, v21\n"
      "v_add_f32 v6, v6, v22\n"
      "v_add_f32 v7, v7, v23\n"
      "ds_read_b128 v[128:131], v201 offset:3200\n"
      "ds_read_b128 v[132:135], v201 offset:3232\n"
      "s_waitcnt lgkmcnt(1)\n"
      "v_mul_f32 v150, v0, v0\n"
      "v_mul_f32 v151, v1, v1\n"
      "v_fmaak_f32 v150, v150, v144, 0xc0135761\n"
      "v_fmaak_f32 v151, v151, v144, 0xc0135761\n"
      "v_mul_f32 v150, v150, v0\n"
      "v_mul_f32 v151, v151, v1\n"
      "v_exp_f32 v150, v150\n"
      "v_exp_f32 v151, v151\n"
      "v_fma_f32 v150, v150, v128, v128\n"
      "v_fma_f32 v151, v151, v129, v129\n"
      "v_rcp_f32 v150, v150\n"
      "v_rcp_f32 v151, v151\n"
      "v_fmac_f32 v40, v0, v150\n"
      "v_fmac_f32 v41, v1, v151\n"
      "v_mul_f32 v150, v2, v2\n"
      "v_mul_f32 v151, v3, v3\n"
      "v_fmaak_f32 v150, v150, v144, 0xc0135761\n"
      "v_fmaak_f32 v151, v151, v144, 0xc0135761\n"
      "v_mul_f32 v150, v150, v2\n"
      "v_mul_f32 v151, v151, v3\n"
      "v_exp_f32 v150, v150\n"
      "v_exp_f32 v151, v151\n"
      "v_fma_f32 v150, v150, v130, v130\n"
      "v_fma_f32 v151, v151, v131, v131\n"
      "v_rcp_f32 v150, v150\n"
      "v_rcp_f32 v151, v151\n"
      "v_fmac_f32 v40, v2, v150\n"
      "v_fmac_f32 v41, v3, v151\n"
      "s_waitcnt lgkmcnt(0)\n"
      "v_mul_f32 v150, v4, v4\n"
      "v_mul_f32 v151, v5, v5\n"
      "v_fmaak_f32 v150, v150, v144, 0xc0135761\n"
      "v_fmaak_f32 v151, v151, v144, 0xc0135761\n"
      "v_mul_f32 v150, v150, v4\n"
      "v_mul_f32 v151, v151, v5\n"
      "v_exp_f32 v150, v150\n"
      "v_exp_f32 v151, v151\n"
      "v_fma_f32 v150, v150, v132, v132\n"
      "v_fma_f32 v151, v151, v133, v133\n"
      "v_rcp_f32 v150, v150\n"
      "v_rcp_f32 v151, v151\n"
      "v_fmac_f32 v40, v4, v150\n"
      "v_fmac_f32 v41, v5, v151\n"
      "v_mul_f32 v150, v6, v6\n"
      "v_mul_f32 v151, v7, v7\n"
      "v_fmaak_f32 v150, v150, v144, 0xc0135761\n"
      "v_fmaak_f32 v151, v151, v144, 0xc0135761\n"
      "v_mul_f32 v150, v150, v6\n"
      "v_mul_f32 v151, v151, v7\n"
      "v_exp_f32 v150, v150\n"
      "v_exp_f32 v151, v151\n"
      "v_fma_f32 v150, v150, v134, v134\n"
      "v_fma_f32 v151, v151, v135, v135\n"
      "v_rcp_f32 v150, v150\n"
      "v_rcp_f32 v151, v151\n"
      "v_fmac_f32 v40, v6, v150\n"
      "v_fmac_f32 v41, v7, v151\n"
      "v_add_f32 v40, v40, v41\n"
      "s_cmp_ge_u32 %[wv], 2\n"
      "s_cselect_b64 vcc, -1, 0\n"
      "v_add_f32 v145, v145, v146\n"
      "v_add_f32 v147, v147, v148\n"
      "v_cndmask_b32 v41, 0, v40, vcc\n"
      "v_cndmask_b32_e64 v40, v40, 0, vcc\n"
      "v_add_f32 v145, v145, v40\n"
      "v_add_f32 v147, v147, v41\n"
      "s_lshl_b32 s57, %[wv], 9\n"
      "s_add_u32 s57, s57, 104256\n"
      "v_lshl_add_u32 v185, v206, 2, s57\n"
      "ds_write_b32 v185, v145\n"
      "ds_write_b32 v185, v147 offset:256\n"
      "v_mov_b32 v202, 1\n"
      "s_mov_b64 exec, 1\n"
      "ds_add_u32 v199, v202 offset:40\n"
      "s_mov_b64 exec, -1\n"
      "s_or_b32 s57, %[wv], %[blk]\n"
      "s_cmp_lg_u32 s57, 0\n"
      "s_cbranch_scc1 8f\n"
      "ds_read_b64 v[0:1], v199 offset:56\n"
      "v_lshlrev_b32 v2, 7, v206\n"
      "s_mov_b32 s57, 0x40000\n"
      "v_add_u32 v3, 8192, v2\n"
      "v_add_u32 v4, 16384, v2\n"
      "v_add_u32 v5, 24576, v2\n"
      "6:\n"
      "global_load_dwordx2 v[6:7], v2, %[wak] sc1\n"
      "global_load_dwordx2 v[8:9], v3, %[wak] sc1\n"
      "global_load_dwordx2 v[10:11], v4, %[wak] sc1\n"
      "global_load_dwordx2 v[12:13], v5, %[wak] sc1\n"
      "s_waitcnt vmcnt(0) lgkmcnt(0)\n"
      "v_cmp_ne_u64_e64 s[40:41], v[0:1], v[6:7]\n"
      "v_cmp_ne_u64_e64 s[42:43], v[0:1], v[8:9]\n"
      "v_cmp_ne_u64_e64 s[44:45], v[0:1], v[10:11]\n"
      "v_cmp_ne_u64_e64 s[46:47], v[0:1], v[12:13]\n"
      "s_or_b64 s[40:41], s[40:41], s[42:43]\n"
      "s_or_b64 s[44:45], s[44:45], s[46:47]\n"
      "s_or_b64 s[40:41], s[40:41], s[44:45]\n"
      "s_cbranch_scc0 7f\n"
      "s_sleep 8\n"
      "s_sub_u32 s57, s57, 1\n"
      "s_cmp_lg_u32 s57, 0\n"
      "s_cbranch_scc1 6b\n"
      "7:\n"
      "v_mov_b32 v2, 0\n"
      "s_mov_b64 exec, 1\n"
      "global_store_dwordx2 v2, v[0:1], %[wep] sc1\n"
      "s_mov_b64 exec, -1\n"
      "8:\n"
      "s_waitcnt vmcnt(0) lgkmcnt(0)\n"
      "s_mov_b32 m0, s58\n"
      "s_endpgm\n"
      "9:\n"
      :
      : [idx] "s"(idx + blk * 1664), [w1] "s"(w1), [b1] "s"(b1), [w2] "s"(w2), [wv] "s"(wave), [rot] "s"(rot), [tab] "s"(table), [blk] "s"(blk),
        [wep] "s"(ws + WS_EPOCH), [wak] "s"(ws + WS_ACK), [wclo] "s"((unsigned)w3p), [wchi] "s"((unsigned)(w3p >> 32)), [wco] "s"(wco), [slt] "s"(SLOT_TAB)
      : "v0", "v1", "v2", "v3", "v4", "v5", "v6", "v7", "v8", "v9", "v10", "v11", "v12", "v13", "v14", "v15", "v16", "v17", "v18", "v19", "v20", "v21", "v22", "v23", "v24", "v25", "v26", "v27", "v28", "v29", "v30", "v31", "v32", "v33", "v34", "v35", "v36", "v37", "v38", "v39", "v40", "v41", "v42", "v43", "v44", "v45", "v46", "v47", "v48", "v49", "v50", "v51", "v52", "v53", "v54", "v55", "v56", "v57", "v58", "v59", "v60", "v61", "v62", "v63", "v64", "v65", "v66", "v67", "v68", "v69", "v70", "v71", "v72", "v73", "v74", "v75", "v76", "v77", "v78", "v79", "v80", "v81", "v82", "v83", "v84", "v85", "v86", "v87", "v88", "v89", "v90", "v91", "v92", "v93", "v94", "v95", "v96", "v97", "v98", "v99", "v100", "v101", "v102", "v103", "v104", "v105", "v106", "v107", "v108", "v109", "v110", "v111", "v112", "v113", "v114", "v115", "v116", "v117", "v118", "v119", "v120", "v121", "v122", "v123", "v124", "v125", "v126", "v127", "v128", "v129", "v130", "v131", "v132", "v133", "v134", "v135", "v136", "v137", "v138", "v139", "v140", "v141", "v142", "v143", "v144", "v145", "v146", "v147", "v148", "v149", "v150", "v151", "v152", "v153", "v154", "v155", "v156", "v157", "v158", "v159", "v160", "v161", "v162", "v163", "v164", "v165", "v166", "v167", "v168", "v169", "v170", "v171", "v172", "v173", "v174", "v175", "v176", "v177", "v178", "v179", "v180", "v181", "v182", "v183", "v184", "v185", "v186", "v187", "v188", "v189", "v190", "v191", "v192", "v193", "v194", "v195", "v196", "v197", "v198", "v199", "v200", "v201", "v202", "v203", "v204", "v205", "v206", "v207", "v208", "v209", "v210", "v211", "v212", "v213", "v214", "v215", "v216", "v217", "v218", "v219", "v220", "v221", "v222", "v223", "v224", "v225", "v226", "v227", "v228", "v229", "v230", "v231", "v232", "v233", "v234", "v235", "v236", "v237", "v238", "v239", "v240", "v241", "v242", "v243", "v244", "v245", "v246", "v247", "v248", "v249", "v250", "v251", "v252", "v253", "v254", "v255", "s40", "s41", "s42", "s43", "s44", "s45", "s46", "s47", "s48", "s49", "s50", "s51", "s52", "s53", "s54", "s55", "s56", "s57", "s58", "s59", "s72", "s73", "s74", "s75", "s76", "s77", "s78", "s79", "vcc", "scc", "memory");
  }
  {
    int lane = __builtin_amdgcn_mbcnt_hi(-1, __builtin_amdgcn_mbcnt_lo(-1, 0));
    asm volatile("" : "+v"(lane));
    const int c = wave - 4, ct = c * 64 + lane;
    u64 ep = 0; int ex; CinIn in; unsigned tb1;
    ex = cin_pre(in, ct, c, lane, blk, bo, ws, ep, tb1);
    asm volatile(
      "s_mov_b32 s64, m0\n"
      "s_mov_b32 s61, %[pbase]\n"
      "s_mov_b32 s60, %[pbase]\n"
      "s_mov_b32 s62, 4\n"
      "s_mov_b32 s70, 0x07060302\n"
      "s_mov_b64 s[72:73], %[ws]\n"
      "s_and_b32 s73, s73, 0xffff\n"
      "s_mov_b32 s74, 39936\n"
      "s_mov_b32 s75, 0x20000\n"
      "s_mov_b32 s71, 19968\n"
      "v_mov_b32 v198, 61376\n"
      "v_mov_b32 v199, 1\n"
      "30:\n"
      "s_memtime s[68:69]\n"
      "s_waitcnt lgkmcnt(0)\n"
      "s_sub_u32 s68, s68, %[tb1]\n"
      "s_cmp_ge_u32 s68, 3000\n"
      "s_cbranch_scc1 31f\n"
      "s_sleep 1\n"
      "s_branch 30b\n"
      "31:\n"
      "buffer_load_dwordx4 v[140:143], v189, s[72:75], 0 offen offset:0 sc1\n"
      "buffer_load_dwordx4 v[144:147], v189, s[72:75], 0 offen offset:1024 sc1\n"
      "buffer_load_dwordx4 v[148:151], v189, s[72:75], 0 offen offset:2048 sc1\n"
      "buffer_load_dwordx4 v[152:155], v189, s[72:75], 0 offen offset:3072 sc1\n"
      "buffer_load_dwordx4 v[156:159], v190, s[72:75], 0 offen offset:0 sc1\n"
      "s_ashr_i32 s63, %[ex2], 1\n"
      "s_add_u32 s63, s63, 112\n"
      "s_mul_i32 s63, s63, 0x800080\n"
      "s_mov_b32 s65, 0x7fff7fff\n"
      "v_and_b32 v96, s65, v200\n"
      "v_and_b32 v97, s65, v201\n"
      "v_and_b32 v98, s65, v202\n"
      "v_and_b32 v99, s65, v203\n"
      "v_pk_sub_u16 v96, v96, s63 clamp\n"
      "v_pk_sub_u16 v97, v97, s63 clamp\n"
      "v_pk_sub_u16 v98, v98, s63 clamp\n"
      "v_pk_sub_u16 v99, v99, s63 clamp\n"
      "v_pk_lshlrev_b16 v96, 3, v96 op_sel_hi:[0,1]\n"
      "v_pk_lshlrev_b16 v97, 3, v97 op_sel_hi:[0,1]\n"
      "v_pk_lshlrev_b16 v98, 3, v98 op_sel_hi:[0,1]\n"
      "v_pk_lshlrev_b16 v99, 3, v99 op_sel_hi:[0,1]\n"
      "v_bfi_b32 v96, s65, v96, v200\n"
      "v_bfi_b32 v97, s65, v97, v201\n"
      "v_bfi_b32 v98, s65, v98, v202\n"
      "v_bfi_b32 v99, s65, v99, v203\n"
      "ds_write_b128 v193, v[96:99] offset:0\n"
      "v_and_b32 v100, s65, v204\n"
      "v_and_b32 v101, s65, v205\n"
      "v_and_b32 v102, s65, v206\n"
      "v_and_b32 v103, s65, v207\n"
      "v_pk_sub_u16 v100, v100, s63 clamp\n"
      "v_pk_sub_u16 v101, v101, s63 clamp\n"
      "v_pk_sub_u16 v102, v102, s63 clamp\n"
      "v_pk_sub_u16 v103, v103, s63 clamp\n"
      "v_pk_lshlrev_b16 v100, 3, v100 op_sel_hi:[0,1]\n"
      "v_pk_lshlrev_b16 v101, 3, v101 op_sel_hi:[0,1]\n"
      "v_pk_lshlrev_b16 v102, 3, v102 op_sel_hi:[0,1]\n"
      "v_pk_lshlrev_b16 v103, 3, v103 op_sel_hi:[0,1]\n"
      "v_bfi_b32 v100, s65, v100, v204\n"
      "v_bfi_b32 v101, s65, v101, v205\n"
      "v_bfi_b32 v102, s65, v102, v206\n"
      "v_bfi_b32 v103, s65, v103, v207\n"
      "ds_write_b128 v193, v[100:103] offset:4096\n"
      "v_and_b32 v96, s65, v208\n"
      "v_and_b32 v97, s65, v209\n"
      "v_and_b32 v98, s65, v210\n"
      "v_and_b32 v99, s65, v211\n"
      "v_pk_sub_u16 v96, v96, s63 clamp\n"
      "v_pk_sub_u16 v97, v97, s63 clamp\n"
      "v_pk_sub_u16 v98, v98, s63 clamp\n"
      "v_pk_sub_u16 v99, v99, s63 clamp\n"
      "v_pk_lshlrev_b16 v96, 3, v96 op_sel_hi:[0,1]\n"
      "v_pk_lshlrev_b16 v97, 3, v97 op_sel_hi:[0,1]\n"
      "v_pk_lshlrev_b16 v98, 3, v98 op_sel_hi:[0,1]\n"
      "v_pk_lshlrev_b16 v99, 3, v99 op_sel_hi:[0,1]\n"
      "v_bfi_b32 v96, s65, v96, v208\n"
      "v_bfi_b32 v97, s65, v97, v209\n"
      "v_bfi_b32 v98, s65, v98, v210\n"
      "v_bfi_b32 v99, s65, v99, v211\n"
      "ds_write_b128 v193, v[96:99] offset:8192\n"
      "v_and_b32 v100, s65, v212\n"
      "v_and_b32 v101, s65, v213\n"
      "v_and_b32 v102, s65, v214\n"
      "v_and_b32 v103, s65, v215\n"
      "v_pk_sub_u16 v100, v100, s63 clamp\n"
      "v_pk_sub_u16 v101, v101, s63 clamp\n"
      "v_pk_sub_u16 v102, v102, s63 clamp\n"
      "v_pk_sub_u16 v103, v103, s63 clamp\n"
      "v_pk_lshlrev_b16 v100, 3, v100 op_sel_hi:[0,1]\n"
      "v_pk_lshlrev_b16 v101, 3, v101 op_sel_hi:[0,1]\n"
      "v_pk_lshlrev_b16 v102, 3, v102 op_sel_hi:[0,1]\n"
      "v_pk_lshlrev_b16 v103, 3, v103 op_sel_hi:[0,1]\n"
      "v_bfi_b32 v100, s65, v100, v212\n"
      "v_bfi_b32 v101, s65, v101, v213\n"
      "v_bfi_b32 v102, s65, v102, v214\n"
      "v_bfi_b32 v103, s65, v103, v215\n"
      "ds_write_b128 v193, v[100:103] offset:12288\n"
      "v_and_b32 v96, s65, v216\n"
      "v_and_b32 v97, s65, v217\n"
      "v_and_b32 v98, s65, v218\n"
      "v_and_b32 v99, s65, v219\n"
      "v_pk_sub_u16 v96, v96, s63 clamp\n"
      "v_pk_sub_u16 v97, v97, s63 clamp\n"
      "v_pk_sub_u16 v98, v98, s63 clamp\n"
      "v_pk_sub_u16 v99, v99, s63 clamp\n"
      "v_pk_lshlrev_b16 v96, 3, v96 op_sel_hi:[0,1]\n"
      "v_pk_lshlrev_b16 v97, 3, v97 op_sel_hi:[0,1]\n"
      "v_pk_lshlrev_b16 v98, 3, v98 op_sel_hi:[0,1]\n"
      "v_pk_lshlrev_b16 v99, 3, v99 op_sel_hi:[0,1]\n"
      "v_bfi_b32 v96, s65, v96, v216\n"
      "v_bfi_b32 v97, s65, v97, v217\n"
      "v_bfi_b32 v98, s65, v98, v218\n"
      "v_bfi_b32 v99, s65, v99, v219\n"
      "ds_write_b128 v193, v[96:99] offset:16384\n"
      "v_and_b32 v100, s65, v220\n"
      "v_and_b32 v101, s65, v221\n"
      "v_and_b32 v102, s65, v222\n"
      "v_and_b32 v103, s65, v223\n"
      "v_pk_sub_u16 v100, v100, s63 clamp\n"
      "v_pk_sub_u16 v101, v101, s63 clamp\n"
      "v_pk_sub_u16 v102, v102, s63 clamp\n"
      "v_pk_sub_u16 v103, v103, s63 clamp\n"
      "v_pk_lshlrev_b16 v100, 3, v100 op_sel_hi:[0,1]\n"
      "v_pk_lshlrev_b16 v101, 3, v101 op_sel_hi:[0,1]\n"
      "v_pk_lshlrev_b16 v102, 3, v102 op_sel_hi:[0,1]\n"
      "v_pk_lshlrev_b16 v103, 3, v103 op_sel_hi:[0,1]\n"
      "v_bfi_b32 v100, s65, v100, v220\n"
      "v_bfi_b32 v101, s65, v101, v221\n"
      "v_bfi_b32 v102, s65, v102, v222\n"
      "v_bfi_b32 v103, s65, v103, v223\n"
      "ds_write_b128 v193, v[100:103] offset:20480\n"
      "v_and_b32 v96, s65, v224\n"
      "v_and_b32 v97, s65, v225\n"
      "v_and_b32 v98, s65, v226\n"
      "v_and_b32 v99, s65, v227\n"
      "v_pk_sub_u16 v96, v96, s63 clamp\n"
      "v_pk_sub_u16 v97, v97, s63 clamp\n"
      "v_pk_sub_u16 v98, v98, s63 clamp\n"
      "v_pk_sub_u16 v99, v99, s63 clamp\n"
      "v_pk_lshlrev_b16 v96, 3, v96 op_sel_hi:[0,1]\n"
      "v_pk_lshlrev_b16 v97, 3, v97 op_sel_hi:[0,1]\n"
      "v_pk_lshlrev_b16 v98, 3, v98 op_sel_hi:[0,1]\n"
      "v_pk_lshlrev_b16 v99, 3, v99 op_sel_hi:[0,1]\n"
      "v_bfi_b32 v96, s65, v96, v224\n"
      "v_bfi_b32 v97, s65, v97, v225\n"
      "v_bfi_b32 v98, s65, v98, v226\n"
      "v_bfi_b32 v99, s65, v99, v227\n"
      "ds_write_b128 v193, v[96:99] offset:24576\n"
      "v_and_b32 v100, s65, v228\n"
      "v_and_b32 v101, s65, v229\n"
      "v_and_b32 v102, s65, v230\n"
      "v_and_b32 v103, s65, v231\n"
      "v_pk_sub_u16 v100, v100, s63 clamp\n"
      "v_pk_sub_u16 v101, v101, s63 clamp\n"
      "v_pk_sub_u16 v102, v102, s63 clamp\n"
      "v_pk_sub_u16 v103, v103, s63 clamp\n"
      "v_pk_lshlrev_b16 v100, 3, v100 op_sel_hi:[0,1]\n"
      "v_pk_lshlrev_b16 v101, 3, v101 op_sel_hi:[0,1]\n"
      "v_pk_lshlrev_b16 v102, 3, v102 op_sel_hi:[0,1]\n"
      "v_pk_lshlrev_b16 v103, 3, v103 op_sel_hi:[0,1]\n"
      "v_bfi_b32 v100, s65, v100, v228\n"
      "v_bfi_b32 v101, s65, v101, v229\n"
      "v_bfi_b32 v102, s65, v102, v230\n"
      "v_bfi_b32 v103, s65, v103, v231\n"
      "s_mov_b64 exec, %[lm]\n"
      "ds_write_b128 v193, v[100:103] offset:28672\n"
      "s_mov_b64 exec, -1\n"
      "s_mov_b32 s65, 0x40000\n"
      "32:\n"
      "s_waitcnt vmcnt(4)\n"
      "v_xor_b32 v140, %[e0], v140\n"
      "v_xor_b32 v141, %[e1], v141\n"
      "v_xor_b32 v142, %[e2], v142\n"
      "v_xor_b32 v143, %[e3], v143\n"
      "v_or3_b32 v196, v140, v141, 0\n"
      "v_or3_b32 v197, v142, v143, 0\n"
      "s_waitcnt vmcnt(3)\n"
      "v_xor_b32 v144, %[e0], v144\n"
      "v_xor_b32 v145, %[e1], v145\n"
      "v_xor_b32 v146, %[e2], v146\n"
      "v_xor_b32 v147, %[e3], v147\n"
      "v_or3_b32 v196, v144, v145, v196\n"
      "v_or3_b32 v197, v146, v147, v197\n"
      "s_waitcnt vmcnt(2)\n"
      "v_xor_b32 v148, %[e0], v148\n"
      "v_xor_b32 v149, %[e1], v149\n"
      "v_xor_b32 v150, %[e2], v150\n"
      "v_xor_b32 v151, %[e3], v151\n"
      "v_or3_b32 v196, v148, v149, v196\n"
      "v_or3_b32 v197, v150, v151, v197\n"
      "s_waitcnt vmcnt(1)\n"
      "v_xor_b32 v152, %[e0], v152\n"
      "v_xor_b32 v153, %[e1], v153\n"
      "v_xor_b32 v154, %[e2], v154\n"
      "v_xor_b32 v155, %[e3], v155\n"
      "v_or3_b32 v196, v152, v153, v196\n"
      "v_or3_b32 v197, v154, v155, v197\n"
      "s_waitcnt vmcnt(0)\n"
      "v_xor_b32 v156, %[e0], v156\n"
      "v_xor_b32 v157, %[e1], v157\n"
      "v_xor_b32 v158, %[e2], v158\n"
      "v_xor_b32 v159, %[e3], v159\n"
      "v_or3_b32 v196, v156, v157, v196\n"
      "v_or3_b32 v197, v158, v159, v197\n"
      "v_or_b32 v196, v196, v197\n"
      "v_and_b32 v196, 0x30003, v196\n"
      "v_cmp_ne_u32 vcc, 0, v196\n"
      "s_cbranch_vccz 33f\n"
      "s_sleep 8\n"
      "buffer_load_dwordx4 v[140:143], v189, s[72:75], 0 offen offset:0 sc1\n"
      "buffer_load_dwordx4 v[144:147], v189, s[72:75], 0 offen offset:1024 sc1\n"
      "buffer_load_dwordx4 v[148:151], v189, s[72:75], 0 offen offset:2048 sc1\n"
      "buffer_load_dwordx4 v[152:155], v189, s[72:75], 0 offen offset:3072 sc1\n"
      "buffer_load_dwordx4 v[156:159], v190, s[72:75], 0 offen offset:0 sc1\n"
      "s_sub_u32 s65, s65, 1\n"
      "s_cmp_lg_u32 s65, 0\n"
      "s_cbranch_scc1 32b\n"
      "s_waitcnt vmcnt(0)\n"
      "33:\n"
      "ds_write_b128 v184, v[140:143]\n"
      "ds_write_b128 v185, v[144:147]\n"
      "ds_write_b128 v186, v[148:151]\n"
      "ds_write_b128 v187, v[152:155]\n"
      "ds_write_b128 v188, v[156:159]\n"
      "s_mov_b64 exec, 1\n"
      "ds_add_u32 v198, v199 offset:8\n"
      "s_mov_b64 exec, -1\n"
      "ds_read_b128 v[224:227], v254\n"
      "40:\n"
      "s_memtime s[68:69]\n"
      "s_waitcnt lgkmcnt(0)\n"
      "s_sub_u32 s68, s68, %[tb1]\n"
      "s_cmp_ge_u32 s68, 5000\n"
      "s_cbranch_scc1 41f\n"
      "s_sleep 1\n"
      "s_branch 40b\n"
      "41:\n"
      "buffer_load_dwordx4 v[44:47], v189, s[72:75], s71 offen offset:0 sc1\n"
      "buffer_load_dwordx4 v[52:55], v189, s[72:75], s71 offen offset:1024 sc1\n"
      "buffer_load_dwordx4 v[60:63], v189, s[72:75], s71 offen offset:2048 sc1\n"
      "buffer_load_dwordx4 v[68:71], v189, s[72:75], s71 offen offset:3072 sc1\n"
      "buffer_load_dwordx4 v[76:79], v190, s[72:75], s71 offen offset:0 sc1\n"
      "s_branch 211f\n"
      "21:\n"
      "s_sleep 1\n"
      "211:\n"
      "ds_read_b32 v220, v198 offset:8\n"
      "s_waitcnt lgkmcnt(0)\n"
      "v_cmp_gt_u32 vcc, 4, v220\n"
      "s_cbranch_vccnz 21b\n"
      "ds_read_b32 v194, v192 offset:704\n"
      "ds_read_b128 v[0:3], v191 offset:0\n"
      "ds_read_b128 v[8:11], v191 offset:32\n"
      "ds_read_b128 v[16:19], v191 offset:64\n"
      "ds_read_b128 v[24:27], v191 offset:96\n"
      "ds_read_b128 v[32:35], v191 offset:128\n"
      "ds_read_b128 v[40:43], v191 offset:160\n"
      "ds_read_b128 v[48:51], v191 offset:192\n"
      "ds_read_b128 v[56:59], v191 offset:224\n"
      "ds_read_b128 v[64:67], v191 offset:256\n"
      "ds_read_b128 v[72:75], v191 offset:288\n"
      "ds_read_b128 v[80:83], v191 offset:320\n"
      "ds_read_b128 v[88:91], v191 offset:352\n"
      "ds_read_b128 v[96:99], v191 offset:384\n"
      "ds_read_b128 v[104:107], v191 offset:416\n"
      "ds_read_b128 v[112:115], v191 offset:448\n"
      "ds_read_b128 v[120:123], v191 offset:480\n"
      "ds_read_b128 v[128:131], v191 offset:512\n"
      "ds_read_b128 v[136:139], v191 offset:544\n"
      "ds_read_b128 v[144:147], v191 offset:576\n"
      "ds_read_b128 v[152:155], v191 offset:608\n"
      "ds_read_b128 v[160:163], v191 offset:640\n"
      "ds_read_b128 v[168:171], v191 offset:672\n"
      "s_mov_b64 exec, 1\n"
      "ds_add_u32 v198, v199 offset:12\n"
      "s_mov_b64 exec, -1\n"
      "v_xor_b32_sdwa v232, v224, v255 dst_sel:DWORD dst_unused:UNUSED_PAD src0_sel:WORD_0 src1_sel:DWORD\n"
      "ds_read_b64_tr_b16 v[232:233], v232\n"
      "v_xor_b32_sdwa v234, v224, v255 dst_sel:DWORD dst_unused:UNUSED_PAD src0_sel:WORD_1 src1_sel:DWORD\n"
      "ds_read_b64_tr_b16 v[234:235], v234\n"
      "v_xor_b32_sdwa v236, v225, v255 dst_sel:DWORD dst_unused:UNUSED_PAD src0_sel:WORD_0 src1_sel:DWORD\n"
      "ds_read_b64_tr_b16 v[236:237], v236\n"
      "v_xor_b32_sdwa v238, v225, v255 dst_sel:DWORD dst_unused:UNUSED_PAD src0_sel:WORD_1 src1_sel:DWORD\n"
      "ds_read_b64_tr_b16 v[238:239], v238\n"
      "v_xor_b32_sdwa v240, v226, v255 dst_sel:DWORD dst_unused:UNUSED_PAD src0_sel:WORD_0 src1_sel:DWORD\n"
      "ds_read_b64_tr_b16 v[240:241], v240\n"
      "v_xor_b32_sdwa v242, v226, v255 dst_sel:DWORD dst_unused:UNUSED_PAD src0_sel:WORD_1 src1_sel:DWORD\n"
      "ds_read_b64_tr_b16 v[242:243], v242\n"
      "v_xor_b32_sdwa v244, v227, v255 dst_sel:DWORD dst_unused:UNUSED_PAD src0_sel:WORD_0 src1_sel:DWORD\n"
      "ds_read_b64_tr_b16 v[244:245], v244\n"
      "s_mov_b32 s65, 0x40000\n"
      "42:\n"
      "s_waitcnt vmcnt(4)\n"
      "v_xor_b32 v44, %[e0], v44\n"
      "v_xor_b32 v45, %[e1], v45\n"
      "v_xor_b32 v46, %[e2], v46\n"
      "v_xor_b32 v47, %[e3], v47\n"
      "v_or3_b32 v196, v44, v45, 0\n"
      "v_or3_b32 v197, v46, v47, 0\n"
      "s_waitcnt vmcnt(3)\n"
      "v_xor_b32 v52, %[e0], v52\n"
      "v_xor_b32 v53, %[e1], v53\n"
      "v_xor_b32 v54, %[e2], v54\n"
      "v_xor_b32 v55, %[e3], v55\n"
      "v_or3_b32 v196, v52, v53, v196\n"
      "v_or3_b32 v197, v54, v55, v197\n"
      "s_waitcnt vmcnt(2)\n"
      "v_xor_b32 v60, %[e0], v60\n"
      "v_xor_b32 v61, %[e1], v61\n"
      "v_xor_b32 v62, %[e2], v62\n"
      "v_xor_b32 v63, %[e3], v63\n"
      "v_or3_b32 v196, v60, v61, v196\n"
      "v_or3_b32 v197, v62, v63, v197\n"
      "s_waitcnt vmcnt(1)\n"
      "v_xor_b32 v68, %[e0], v68\n"
      "v_xor_b32 v69, %[e1], v69\n"
      "v_xor_b32 v70, %[e2], v70\n"
      "v_xor_b32 v71, %[e3], v71\n"
      "v_or3_b32 v196, v68, v69, v196\n"
      "v_or3_b32 v197, v70, v71, v197\n"
      "s_waitcnt vmcnt(0)\n"
      "v_xor_b32 v76, %[e0], v76\n"
      "v_xor_b32 v77, %[e1], v77\n"
      "v_xor_b32 v78, %[e2], v78\n"
      "v_xor_b32 v79, %[e3], v79\n"
      "v_or3_b32 v196, v76, v77, v196\n"
      "v_or3_b32 v197, v78, v79, v197\n"
      "v_or_b32 v196, v196, v197\n"
      "v_and_b32 v196, 0x30003, v196\n"
      "v_cmp_ne_u32 vcc, 0, v196\n"
      "s_cbranch_vccz 43f\n"
      "s_sleep 8\n"
      "buffer_load_dwordx4 v[44:47], v189, s[72:75], s71 offen offset:0 sc1\n"
      "buffer_load_dwordx4 v[52:55], v189, s[72:75], s71 offen offset:1024 sc1\n"
      "buffer_load_dwordx4 v[60:63], v189, s[72:75], s71 offen offset:2048 sc1\n"
      "buffer_load_dwordx4 v[68:71], v189, s[72:75], s71 offen offset:3072 sc1\n"
      "buffer_load_dwordx4 v[76:79], v190, s[72:75], s71 offen offset:0 sc1\n"
      "s_sub_u32 s65, s65, 1\n"
      "s_cmp_lg_u32 s65, 0\n"
      "s_cbranch_scc1 42b\n"
      "s_waitcnt vmcnt(0)\n"
      "43:\n"
      "s_branch 231f\n"
      "23:\n"
      "s_sleep 1\n"
      "231:\n"
      "ds_read_b32 v220, v198 offset:12\n"
      "s_waitcnt lgkmcnt(0)\n"
      "v_cmp_gt_u32 vcc, 4, v220\n"
      "s_cbranch_vccnz 23b\n"
      "ds_write_b128 v184, v[44:47]\n"
      "ds_write_b128 v185, v[52:55]\n"
      "ds_write_b128 v186, v[60:63]\n"
      "ds_write_b128 v187, v[68:71]\n"
      "ds_write_b128 v188, v[76:79]\n"
      "s_mov_b64 exec, 1\n"
      "ds_add_u32 v198, v199 offset:16\n"
      "s_mov_b64 exec, -1\n"
      "v_pk_mul_f16 v246, v233, v232 op_sel:[0,0] op_sel_hi:[1,0]\n"
      "v_pk_mul_f16 v247, v233, v232 op_sel:[0,1] op_sel_hi:[1,1]\n"
      "v_pk_mul_f16 v248, v234, v232 op_sel:[0,0] op_sel_hi:[1,0]\n"
      "v_pk_mul_f16 v249, v234, v232 op_sel:[0,1] op_sel_hi:[1,1]\n"
      "v_pk_mul_f16 v250, v235, v232 op_sel:[0,0] op_sel_hi:[1,0]\n"
      "v_pk_mul_f16 v251, v235, v232 op_sel:[0,1] op_sel_hi:[1,1]\n"
      "s_branch 241f\n"
      "24:\n"
      "s_sleep 1\n"
      "241:\n"
      "ds_read_b32 v220, v198 offset:16\n"
      "s_waitcnt lgkmcnt(0)\n"
      "v_cmp_gt_u32 vcc, 4, v220\n"
      "s_cbranch_vccnz 24b\n"
      "s_setprio 1\n"
      "ds_read_b32 v196, v192 offset:764\n"
      "ds_read_b128 v[176:179], v191 offset:704\n"
      "ds_read_b128 v[180:183], v191 offset:736\n"
      "ds_read_b128 v[4:7], v191 offset:0\n"
      "ds_read_b128 v[12:15], v191 offset:32\n"
      "ds_read_b128 v[20:23], v191 offset:64\n"
      "ds_read_b128 v[28:31], v191 offset:96\n"
      "ds_read_b128 v[36:39], v191 offset:128\n"
      "ds_read_b128 v[44:47], v191 offset:160\n"
      "ds_read_b128 v[52:55], v191 offset:192\n"
      "ds_read_b128 v[60:63], v191 offset:224\n"
      "ds_read_b128 v[68:71], v191 offset:256\n"
      "ds_read_b128 v[76:79], v191 offset:288\n"
      "ds_read_b128 v[84:87], v191 offset:320\n"
      "ds_read_b128 v[92:95], v191 offset:352\n"
      "s_waitcnt lgkmcnt(14)\n"
      "v_bfe_i32 v194, v194, 2, 14\n"
      "v_bfe_i32 v196, v196, 2, 14\n"
      "v_add_u32 v221, v194, v196\n"
      "v_mov_b32 v220, v221\n"
      "s_nop 1\n"
      "v_max_i32_dpp v220, v220, v220 quad_perm:[1,0,3,2] row_mask:0xf bank_mask:0xf\n"
      "ds_read_b128 v[100:103], v191 offset:384\n"
      "ds_read_b128 v[108:111], v191 offset:416\n"
      "v_max_i32_dpp v220, v220, v220 quad_perm:[2,3,0,1] row_mask:0xf bank_mask:0xf\n"
      "ds_read_b128 v[116:119], v191 offset:448\n"
      "ds_read_b128 v[124:127], v191 offset:480\n"
      "v_max_i32_dpp v220, v220, v220 row_half_mirror row_mask:0xf bank_mask:0xf\n"
      "ds_read_b128 v[132:135], v191 offset:512\n"
      "ds_read_b128 v[140:143], v191 offset:544\n"
      "v_max_i32_dpp v220, v220, v220 row_mirror row_mask:0xf bank_mask:0xf\n"
      "ds_read_b128 v[148:151], v191 offset:576\n"
      "ds_read_b128 v[156:159], v191 offset:608\n"
      "v_readlane_b32 s63, v220, 0\n"
      "v_readlane_b32 s65, v220, 16\n"
      "ds_read_b128 v[164:167], v191 offset:640\n"
      "ds_read_b128 v[172:175], v191 offset:672\n"
      "s_max_i32 s63, s63, s65\n"
      "v_subrev_u32 v221, s63, v221\n"
      "v_max_i32 v221, -40, v221\n"
      "v_ldexp_f32 v221, 1.0, v221\n"
      "v_cvt_f16_f32 v221, v221\n"
      "s_add_i32 s63, s63, %[ex2]\n"
      "v_mov_b32 v220, s63\n"
      "s_mov_b64 exec, 1\n"
      "ds_write_b32 v198, v220 offset:48\n"
      "s_mov_b64 exec, -1\n"
      "v_cmp_neq_f16 vcc, 1.0, v221\n"
      "s_cbranch_vccz 27f\n"
      "v_pk_mul_f16 v0, v0, v221 op_sel_hi:[1,0]\n"
      "v_pk_mul_f16 v1, v1, v221 op_sel_hi:[1,0]\n"
      "v_pk_mul_f16 v2, v2, v221 op_sel_hi:[1,0]\n"
      "v_pk_mul_f16 v3, v3, v221 op_sel_hi:[1,0]\n"
      "v_pk_mul_f16 v8, v8, v221 op_sel_hi:[1,0]\n"
      "v_pk_mul_f16 v9, v9, v221 op_sel_hi:[1,0]\n"
      "v_pk_mul_f16 v10, v10, v221 op_sel_hi:[1,0]\n"
      "v_pk_mul_f16 v11, v11, v221 op_sel_hi:[1,0]\n"
      "v_pk_mul_f16 v16, v16, v221 op_sel_hi:[1,0]\n"
      "v_pk_mul_f16 v17, v17, v221 op_sel_hi:[1,0]\n"
      "v_pk_mul_f16 v18, v18, v221 op_sel_hi:[1,0]\n"
      "v_pk_mul_f16 v19, v19, v221 op_sel_hi:[1,0]\n"
      "v_pk_mul_f16 v24, v24, v221 op_sel_hi:[1,0]\n"
      "v_pk_mul_f16 v25, v25, v221 op_sel_hi:[1,0]\n"
      "v_pk_mul_f16 v26, v26, v221 op_sel_hi:[1,0]\n"
      "v_pk_mul_f16 v27, v27, v221 op_sel_hi:[1,0]\n"
      "v_pk_mul_f16 v32, v32, v221 op_sel_hi:[1,0]\n"
      "v_pk_mul_f16 v33, v33, v221 op_sel_hi:[1,0]\n"
      "v_pk_mul_f16 v34, v34, v221 op_sel_hi:[1,0]\n"
      "v_pk_mul_f16 v35, v35, v221 op_sel_hi:[1,0]\n"
      "v_pk_mul_f16 v40, v40, v221 op_sel_hi:[1,0]\n"
      "v_pk_mul_f16 v41, v41, v221 op_sel_hi:[1,0]\n"
      "v_pk_mul_f16 v42, v42, v221 op_sel_hi:[1,0]\n"
      "v_pk_mul_f16 v43, v43, v221 op_sel_hi:[1,0]\n"
      "v_pk_mul_f16 v48, v48, v221 op_sel_hi:[1,0]\n"
      "v_pk_mul_f16 v49, v49, v221 op_sel_hi:[1,0]\n"
      "v_pk_mul_f16 v50, v50, v221 op_sel_hi:[1,0]\n"
      "v_pk_mul_f16 v51, v51, v221 op_sel_hi:[1,0]\n"
      "v_pk_mul_f16 v56, v56, v221 op_sel_hi:[1,0]\n"
      "v_pk_mul_f16 v57, v57, v221 op_sel_hi:[1,0]\n"
      "v_pk_mul_f16 v58, v58, v221 op_sel_hi:[1,0]\n"
      "v_pk_mul_f16 v59, v59, v221 op_sel_hi:[1,0]\n"
      "v_pk_mul_f16 v64, v64, v221 op_sel_hi:[1,0]\n"
      "v_pk_mul_f16 v65, v65, v221 op_sel_hi:[1,0]\n"
      "v_pk_mul_f16 v66, v66, v221 op_sel_hi:[1,0]\n"
      "v_pk_mul_f16 v67, v67, v221 op_sel_hi:[1,0]\n"
      "v_pk_mul_f16 v72, v72, v221 op_sel_hi:[1,0]\n"
      "v_pk_mul_f16 v73, v73, v221 op_sel_hi:[1,0]\n"
      "v_pk_mul_f16 v74, v74, v221 op_sel_hi:[1,0]\n"
      "v_pk_mul_f16 v75, v75, v221 op_sel_hi:[1,0]\n"
      "v_pk_mul_f16 v80, v80, v221 op_sel_hi:[1,0]\n"
      "v_pk_mul_f16 v81, v81, v221 op_sel_hi:[1,0]\n"
      "v_pk_mul_f16 v82, v82, v221 op_sel_hi:[1,0]\n"
      "v_pk_mul_f16 v83, v83, v221 op_sel_hi:[1,0]\n"
      "v_pk_mul_f16 v88, v88, v221 op_sel_hi:[1,0]\n"
      "v_pk_mul_f16 v89, v89, v221 op_sel_hi:[1,0]\n"
      "v_pk_mul_f16 v90, v90, v221 op_sel_hi:[1,0]\n"
      "v_pk_mul_f16 v91, v91, v221 op_sel_hi:[1,0]\n"
      "v_pk_mul_f16 v96, v96, v221 op_sel_hi:[1,0]\n"
      "v_pk_mul_f16 v97, v97, v221 op_sel_hi:[1,0]\n"
      "v_pk_mul_f16 v98, v98, v221 op_sel_hi:[1,0]\n"
      "v_pk_mul_f16 v99, v99, v221 op_sel_hi:[1,0]\n"
      "v_pk_mul_f16 v104, v104, v221 op_sel_hi:[1,0]\n"
      "v_pk_mul_f16 v105, v105, v221 op_sel_hi:[1,0]\n"
      "v_pk_mul_f16 v106, v106, v221 op_sel_hi:[1,0]\n"
      "v_pk_mul_f16 v107, v107, v221 op_sel_hi:[1,0]\n"
      "v_pk_mul_f16 v112, v112, v221 op_sel_hi:[1,0]\n"
      "v_pk_mul_f16 v113, v113, v221 op_sel_hi:[1,0]\n"
      "v_pk_mul_f16 v114, v114, v221 op_sel_hi:[1,0]\n"
      "v_pk_mul_f16 v115, v115, v221 op_sel_hi:[1,0]\n"
      "v_pk_mul_f16 v120, v120, v221 op_sel_hi:[1,0]\n"
      "v_pk_mul_f16 v121, v121, v221 op_sel_hi:[1,0]\n"
      "v_pk_mul_f16 v122, v122, v221 op_sel_hi:[1,0]\n"
      "v_pk_mul_f16 v123, v123, v221 op_sel_hi:[1,0]\n"
      "v_pk_mul_f16 v128, v128, v221 op_sel_hi:[1,0]\n"
      "v_pk_mul_f16 v129, v129, v221 op_sel_hi:[1,0]\n"
      "v_pk_mul_f16 v130, v130, v221 op_sel_hi:[1,0]\n"
      "v_pk_mul_f16 v131, v131, v221 op_sel_hi:[1,0]\n"
      "v_pk_mul_f16 v136, v136, v221 op_sel_hi:[1,0]\n"
      "v_pk_mul_f16 v137, v137, v221 op_sel_hi:[1,0]\n"
      "v_pk_mul_f16 v138, v138, v221 op_sel_hi:[1,0]\n"
      "v_pk_mul_f16 v139, v139, v221 op_sel_hi:[1,0]\n"
      "v_pk_mul_f16 v144, v144, v221 op_sel_hi:[1,0]\n"
      "v_pk_mul_f16 v145, v145, v221 op_sel_hi:[1,0]\n"
      "v_pk_mul_f16 v146, v146, v221 op_sel_hi:[1,0]\n"
      "v_pk_mul_f16 v147, v147, v221 op_sel_hi:[1,0]\n"
      "v_pk_mul_f16 v152, v152, v221 op_sel_hi:[1,0]\n"
      "v_pk_mul_f16 v153, v153, v221 op_sel_hi:[1,0]\n"
      "v_pk_mul_f16 v154, v154, v221 op_sel_hi:[1,0]\n"
      "v_pk_mul_f16 v155, v155, v221 op_sel_hi:[1,0]\n"
      "v_pk_mul_f16 v160, v160, v221 op_sel_hi:[1,0]\n"
      "v_pk_mul_f16 v161, v161, v221 op_sel_hi:[1,0]\n"
      "v_pk_mul_f16 v162, v162, v221 op_sel_hi:[1,0]\n"
      "v_pk_mul_f16 v163, v163, v221 op_sel_hi:[1,0]\n"
      "v_pk_mul_f16 v168, v168, v221 op_sel_hi:[1,0]\n"
      "v_pk_mul_f16 v169, v169, v221 op_sel_hi:[1,0]\n"
      "v_pk_mul_f16 v170, v170, v221 op_sel_hi:[1,0]\n"
      "v_pk_mul_f16 v171, v171, v221 op_sel_hi:[1,0]\n"
      "27:\n"
      "s_waitcnt lgkmcnt(0)\n"
      "s_mov_b32 exec_lo, 0\n"
      "v_mov_b32 v183, 0\n"
      "s_mov_b32 exec_lo, -1\n"
      "s_nop 1\n"
      "13:\n"
      "v_mfma_f32_32x32x16_f16 v[200:215], v[176:179], v[232:235], 0\n"
      "v_pk_mul_f16 v252, v236, v232 op_sel:[0,0] op_sel_hi:[1,0]\n"
      "v_pk_mul_f16 v253, v236, v232 op_sel:[0,1] op_sel_hi:[1,1]\n"
      "s_mov_b32 m0, s60\n"
      "s_mov_b32 s60, s61\n"
      "s_add_u32 s61, s61, 256\n"
      "v_mfma_f32_32x32x16_f16 v[200:215], v[180:183], v[236:239], v[200:215]\n"
      "v_mul_f32 v229, v184, v216\n"
      "v_mul_f32 v230, v185, v217\n"
      "v_fmac_f32 v229, v186, v218\n"
      "v_fmac_f32 v230, v187, v219\n"
      "v_fmac_f32 v229, v188, v220\n"
      "v_mfma_f32_32x32x16_f16 v[200:215], v[4:7], v[246:249], v[200:215]\n"
      "v_fmac_f32 v230, v189, v221\n"
      "v_fmac_f32 v229, v190, v222\n"
      "v_fmac_f32 v230, v191, v223\n"
      "v_fmac_f32 v229, v192, v224\n"
      "v_fmac_f32 v230, v193, v225\n"
      "v_mfma_f32_32x32x16_f16 v[200:215], v[12:15], v[250:253], v[200:215]\n"
      "v_fmac_f32 v229, v194, v226\n"
      "v_fmac_f32 v230, v195, v227\n"
      "v_fmac_f32 v229, v196, v228\n"
      "v_add_f32 v229, v229, v230\n"
      "ds_write_addtid_b32 v229\n"
      "v_mfma_f32_32x32x16_f16 v[184:199], v[0:3], v[246:249], 0\n"
      "v_pk_mul_f16 v246, v237, v232 op_sel:[0,0] op_sel_hi:[1,0]\n"
      "v_perm_b32 v247, v234, v233, s70\n"
      "v_pk_mul_f16 v247, v247, v247\n"
      "v_pk_mul_f16 v248, v237, v232 op_sel:[0,1] op_sel_hi:[1,1]\n"
      "v_pk_mul_f16 v249, v238, v232 op_sel:[0,0] op_sel_hi:[1,0]\n"
      "v_mfma_f32_32x32x16_f16 v[184:199], v[8:11], v[250:253], v[184:199]\n"
      "v_pk_mul_f16 v250, v238, v232 op_sel:[0,1] op_sel_hi:[1,1]\n"
      "v_pk_mul_f16 v251, v233, v235 op_sel:[0,0] op_sel_hi:[1,0]\n"
      "v_mfma_f32_32x32x16_f16 v[184:199], v[16:19], v[246:249], v[184:199]\n"
      "v_pk_mul_f16 v252, v233, v235 op_sel:[0,1] op_sel_hi:[1,1]\n"
      "v_pk_mul_f16 v253, v233, v237 op_sel:[0,0] op_sel_hi:[1,0]\n"
      "v_mfma_f32_32x32x16_f16 v[200:215], v[20:23], v[246:249], v[200:215]\n"
      "v_pk_mul_f16 v246, v233, v237 op_sel:[0,1] op_sel_hi:[1,1]\n"
      "v_perm_b32 v247, v236, v235, s70\n"
      "v_pk_mul_f16 v247, v247, v247\n"
      "v_mfma_f32_32x32x16_f16 v[184:199], v[24:27], v[250:253], v[184:199]\n"
      "v_pk_mul_f16 v248, v234, v235 op_sel:[0,0] op_sel_hi:[1,0]\n"
      "v_pk_mul_f16 v249, v234, v235 op_sel:[0,1] op_sel_hi:[1,1]\n"
      "ds_read_b128 v[216:219], v254 offset:256\n"
      "v_mfma_f32_32x32x16_f16 v[200:215], v[28:31], v[250:253], v[200:215]\n"
      "v_pk_mul_f16 v250, v234, v238 op_sel:[0,0] op_sel_hi:[1,0]\n"
      "v_pk_mul_f16 v251, v234, v238 op_sel:[0,1] op_sel_hi:[1,1]\n"
      "v_mfma_f32_32x32x16_f16 v[184:199], v[32:35], v[246:249], v[184:199]\n"
      "v_pk_mul_f16 v252, v236, v237 op_sel:[0,0] op_sel_hi:[1,0]\n"
      "v_pk_mul_f16 v253, v236, v237 op_sel:[0,1] op_sel_hi:[1,1]\n"
      "v_mfma_f32_32x32x16_f16 v[200:215], v[36:39], v[246:249], v[200:215]\n"
      "v_pk_mul_f16 v246, v236, v238 op_sel:[0,0] op_sel_hi:[1,0]\n"
      "v_perm_b32 v247, v238, v237, s70\n"
      "v_pk_mul_f16 v247, v247, v247\n"
      "v_mfma_f32_32x32x16_f16 v[184:199], v[40:43], v[250:253], v[184:199]\n"
      "v_pk_mul_f16 v248, v236, v238 op_sel:[0,1] op_sel_hi:[1,1]\n"
      "v_pk_mul_f16 v249, v232, v232 op_sel:[0,0] op_sel_hi:[1,0]\n"
      "v_mfma_f32_32x32x16_f16 v[200:215], v[44:47], v[250:253], v[200:215]\n"
      "v_pk_mul_f16 v250, v233, v233 op_sel:[0,0] op_sel_hi:[1,0]\n"
      "v_pk_mul_f16 v251, v234, v234 op_sel:[0,0] op_sel_hi:[1,0]\n"
      "v_mfma_f32_32x32x16_f16 v[184:199], v[48:51], v[246:249], v[184:199]\n"
      "v_pk_mul_f16 v252, v235, v235 op_sel:[0,0] op_sel_hi:[1,0]\n"
      "v_pk_mul_f16 v253, v236, v236 op_sel:[0,0] op_sel_hi:[1,0]\n"
      "v_mfma_f32_32x32x16_f16 v[200:215], v[52:55], v[246:249], v[200:215]\n"
      "v_pk_mul_f16 v246, v237, v237 op_sel:[0,0] op_sel_hi:[1,0]\n"
      "v_pk_mul_f16 v247, v238, v238 op_sel:[0,0] op_sel_hi:[1,0]\n"
      "v_mfma_f32_32x32x16_f16 v[184:199], v[56:59], v[250:253], v[184:199]\n"
      "s_waitcnt lgkmcnt(2)\n"
      "v_pk_mul_f16 v248, v233, v240 op_sel:[0,0] op_sel_hi:[1,0]\n"
      "v_pk_mul_f16 v249, v233, v240 op_sel:[0,1] op_sel_hi:[1,1]\n"
      "v_mfma_f32_32x32x16_f16 v[200:215], v[60:63], v[250:253], v[200:215]\n"
      "v_pk_mul_f16 v250, v233, v241 op_sel:[0,0] op_sel_hi:[1,0]\n"
      "v_pk_mul_f16 v251, v233, v241 op_sel:[0,1] op_sel_hi:[1,1]\n"
      "v_mfma_f32_32x32x16_f16 v[184:199], v[64:67], v[246:249], v[184:199]\n"
      "v_pk_mul_f16 v252, v233, v245 op_sel:[0,0] op_sel_hi:[1,0]\n"
      "v_pk_mul_f16 v253, v233, v245 op_sel:[0,1] op_sel_hi:[1,1]\n"
      "v_mfma_f32_32x32x16_f16 v[200:215], v[68:71], v[246:249], v[200:215]\n"
      "v_pk_mul_f16 v246, v240, v234 op_sel:[0,0] op_sel_hi:[1,0]\n"
      "v_pk_mul_f16 v247, v240, v234 op_sel:[0,1] op_sel_hi:[1,1]\n"
      "v_mfma_f32_32x32x16_f16 v[184:199], v[72:75], v[250:253], v[184:199]\n"
      "v_pk_mul_f16 v248, v240, v238 op_sel:[0,0] op_sel_hi:[1,0]\n"
      "v_pk_mul_f16 v249, v240, v238 op_sel:[0,1] op_sel_hi:[1,1]\n"
      "v_mfma_f32_32x32x16_f16 v[200:215], v[76:79], v[250:253], v[200:215]\n"
      "v_pk_mul_f16 v250, v234, v242 op_sel:[0,0] op_sel_hi:[1,0]\n"
      "v_pk_mul_f16 v251, v234, v242 op_sel:[0,1] op_sel_hi:[1,1]\n"
      "v_mfma_f32_32x32x16_f16 v[184:199], v[80:83], v[246:249], v[184:199]\n"
      "v_pk_mul_f16 v252, v234, v243 op_sel:[0,0] op_sel_hi:[1,0]\n"
      "v_pk_mul_f16 v253, v234, v243 op_sel:[0,1] op_sel_hi:[1,1]\n"
      "v_mfma_f32_32x32x16_f16 v[200:215], v[84:87], v[246:249], v[200:215]\n"
      "v_pk_mul_f16 v246, v235, v241 op_sel:[0,0] op_sel_hi:[1,0]\n"
      "v_pk_mul_f16 v247, v235, v241 op_sel:[0,1] op_sel_hi:[1,1]\n"
      "v_mfma_f32_32x32x16_f16 v[184:199], v[88:91], v[250:253], v[184:199]\n"
      "v_pk_mul_f16 v248, v235, v243 op_sel:[0,0] op_sel_hi:[1,0]\n"
      "v_pk_mul_f16 v249, v235, v243 op_sel:[0,1] op_sel_hi:[1,1]\n"
      "v_mfma_f32_32x32x16_f16 v[200:215], v[92:95], v[250:253], v[200:215]\n"
      "v_pk_mul_f16 v250, v235, v244 op_sel:[0,0] op_sel_hi:[1,0]\n"
      "v_pk_mul_f16 v251, v235, v244 op_sel:[0,1] op_sel_hi:[1,1]\n"
      "v_mfma_f32_32x32x16_f16 v[184:199], v[96:99], v[246:249], v[184:199]\n"
      "v_pk_mul_f16 v252, v241, v236 op_sel:[0,0] op_sel_hi:[1,0]\n"
      "v_pk_mul_f16 v253, v241, v236 op_sel:[0,1] op_sel_hi:[1,1]\n"
      "v_mfma_f32_32x32x16_f16 v[200:215], v[100:103], v[246:249], v[200:215]\n"
      "v_pk_mul_f16 v246, v242, v236 op_sel:[0,0] op_sel_hi:[1,0]\n"
      "v_pk_mul_f16 v247, v242, v236 op_sel:[0,1] op_sel_hi:[1,1]\n"
      "v_mfma_f32_32x32x16_f16 v[184:199], v[104:107], v[250:253], v[184:199]\n"
      "v_pk_mul_f16 v248, v242, v237 op_sel:[0,0] op_sel_hi:[1,0]\n"
      "v_pk_mul_f16 v249, v242, v237 op_sel:[0,1] op_sel_hi:[1,1]\n"
      "v_mfma_f32_32x32x16_f16 v[200:215], v[108:111], v[250:253], v[200:215]\n"
      "v_pk_mul_f16 v250, v243, v237 op_sel:[0,0] op_sel_hi:[1,0]\n"
      "v_pk_mul_f16 v251, v243, v237 op_sel:[0,1] op_sel_hi:[1,1]\n"
      "v_mfma_f32_32x32x16_f16 v[184:199], v[112:115], v[246:249], v[184:199]\n"
      "v_pk_mul_f16 v252, v244, v236 op_sel:[0,0] op_sel_hi:[1,0]\n"
      "v_pk_mul_f16 v253, v244, v236 op_sel:[0,1] op_sel_hi:[1,1]\n"
      "v_mfma_f32_32x32x16_f16 v[200:215], v[116:119], v[246:249], v[200:215]\n"
      "v_pk_mul_f16 v246, v244, v238 op_sel:[0,0] op_sel_hi:[1,0]\n"
      "v_pk_mul_f16 v247, v244, v238 op_sel:[0,1] op_sel_hi:[1,1]\n"
      "v_mfma_f32_32x32x16_f16 v[184:199], v[120:123], v[250:253], v[184:199]\n"
      "v_pk_mul_f16 v248, v237, v245 op_sel:[0,0] op_sel_hi:[1,0]\n"
      "v_pk_mul_f16 v249, v237, v245 op_sel:[0,1] op_sel_hi:[1,1]\n"
      "v_mfma_f32_32x32x16_f16 v[200:215], v[124:127], v[250:253], v[200:215]\n"
      "v_pk_mul_f16 v250, v245, v238 op_sel:[0,0] op_sel_hi:[1,0]\n"
      "v_pk_mul_f16 v251, v245, v238 op_sel:[0,1] op_sel_hi:[1,1]\n"
      "v_mfma_f32_32x32x16_f16 v[184:199], v[128:131], v[246:249], v[184:199]\n"
      "v_pk_mul_f16 v252, v240, v241 op_sel:[0,0] op_sel_hi:[1,0]\n"
      "v_pk_mul_f16 v253, v240, v241 op_sel:[0,1] op_sel_hi:[1,1]\n"
      "s_waitcnt lgkmcnt(0)\n"
      "v_xor_b32_sdwa v232, v216, v255 dst_sel:DWORD dst_unused:UNUSED_PAD src0_sel:WORD_0 src1_sel:DWORD\n"
      "ds_read_b64_tr_b16 v[232:233], v232\n"
      "v_mfma_f32_32x32x16_f16 v[200:215], v[132:135], v[246:249], v[200:215]\n"
      "v_pk_mul_f16 v246, v240, v242 op_sel:[0,0] op_sel_hi:[1,0]\n"
      "v_pk_mul_f16 v247, v240, v242 op_sel:[0,1] op_sel_hi:[1,1]\n"
      "v_xor_b32_sdwa v234, v216, v255 dst_sel:DWORD dst_unused:UNUSED_PAD src0_sel:WORD_1 src1_sel:DWORD\n"
      "ds_read_b64_tr_b16 v[234:235], v234\n"
      "v_mfma_f32_32x32x16_f16 v[184:199], v[136:139], v[250:253], v[184:199]\n"
      "v_pk_mul_f16 v248, v240, v245 op_sel:[0,0] op_sel_hi:[1,0]\n"
      "v_pk_mul_f16 v249, v240, v245 op_sel:[0,1] op_sel_hi:[1,1]\n"
      "v_xor_b32_sdwa v236, v217, v255 dst_sel:DWORD dst_unused:UNUSED_PAD src0_sel:WORD_0 src1_sel:DWORD\n"
      "ds_read_b64_tr_b16 v[236:237], v236\n"
      "v_mfma_f32_32x32x16_f16 v[200:215], v[140:143], v[250:253], v[200:215]\n"
      "v_pk_mul_f16 v250, v241, v242 op_sel:[0,0] op_sel_hi:[1,0]\n"
      "v_pk_mul_f16 v251, v241, v242 op_sel:[0,1] op_sel_hi:[1,1]\n"
      "v_xor_b32_sdwa v238, v217, v255 dst_sel:DWORD dst_unused:UNUSED_PAD src0_sel:WORD_1 src1_sel:DWORD\n"
      "ds_read_b64_tr_b16 v[238:239], v238\n"
      "v_mfma_f32_32x32x16_f16 v[184:199], v[144:147], v[246:249], v[184:199]\n"
      "v_pk_mul_f16 v252, v241, v244 op_sel:[0,0] op_sel_hi:[1,0]\n"
      "v_pk_mul_f16 v253, v241, v244 op_sel:[0,1] op_sel_hi:[1,1]\n"
      "v_mfma_f32_32x32x16_f16 v[200:215], v[148:151], v[246:249], v[200:215]\n"
      "v_pk_mul_f16 v246, v242, v243 op_sel:[0,0] op_sel_hi:[1,0]\n"
      "v_pk_mul_f16 v247, v242, v243 op_sel:[0,1] op_sel_hi:[1,1]\n"
      "v_mfma_f32_32x32x16_f16 v[184:199], v[152:155], v[250:253], v[184:199]\n"
      "v_pk_mul_f16 v248, v243, v244 op_sel:[0,0] op_sel_hi:[1,0]\n"
      "v_pk_mul_f16 v249, v243, v244 op_sel:[0,1] op_sel_hi:[1,1]\n"
      "v_mfma_f32_32x32x16_f16 v[200:215], v[156:159], v[250:253], v[200:215]\n"
      "v_pk_mul_f16 v250, v243, v245 op_sel:[0,0] op_sel_hi:[1,0]\n"
      "v_pk_mul_f16 v251, v243, v245 op_sel:[0,1] op_sel_hi:[1,1]\n"
      "v_mfma_f32_32x32x16_f16 v[184:199], v[160:163], v[246:249], v[184:199]\n"
      "v_pk_mul_f16 v252, v244, v245 op_sel:[0,0] op_sel_hi:[1,0]\n"
      "v_pk_mul_f16 v253, v244, v245 op_sel:[0,1] op_sel_hi:[1,1]\n"
      "v_mfma_f32_32x32x16_f16 v[200:215], v[164:167], v[246:249], v[200:215]\n"
      "s_waitcnt lgkmcnt(0)\n"
      "v_pk_mul_f16 v246, v233, v232 op_sel:[0,0] op_sel_hi:[1,0]\n"
      "v_pk_mul_f16 v247, v233, v232 op_sel:[0,1] op_sel_hi:[1,1]\n"
      "v_xor_b32_sdwa v240, v218, v255 dst_sel:DWORD dst_unused:UNUSED_PAD src0_sel:WORD_0 src1_sel:DWORD\n"
      "ds_read_b64_tr_b16 v[240:241], v240\n"
      "v_mfma_f32_32x32x16_f16 v[184:199], v[168:171], v[250:253], v[184:199]\n"
      "v_pk_mul_f16 v248, v234, v232 op_sel:[0,0] op_sel_hi:[1,0]\n"
      "v_pk_mul_f16 v249, v234, v232 op_sel:[0,1] op_sel_hi:[1,1]\n"
      "v_xor_b32_sdwa v242, v218, v255 dst_sel:DWORD dst_unused:UNUSED_PAD src0_sel:WORD_1 src1_sel:DWORD\n"
      "ds_read_b64_tr_b16 v[242:243], v242\n"
      "v_mfma_f32_32x32x16_f16 v[200:215], v[172:175], v[250:253], v[200:215]\n"
      "v_pk_mul_f16 v250, v235, v232 op_sel:[0,0] op_sel_hi:[1,0]\n"
      "v_pk_mul_f16 v251, v235, v232 op_sel:[0,1] op_sel_hi:[1,1]\n"
      "v_xor_b32_sdwa v244, v219, v255 dst_sel:DWORD dst_unused:UNUSED_PAD src0_sel:WORD_0 src1_sel:DWORD\n"
      "ds_read_b64_tr_b16 v[244:245], v244\n"
      "s_nop 0\n"
      "v_mfma_f32_32x32x16_f16 v[216:231], v[176:179], v[232:235], 0\n"
      "v_pk_mul_f16 v252, v236, v232 op_sel:[0,0] op_sel_hi:[1,0]\n"
      "v_pk_mul_f16 v253, v236, v232 op_sel:[0,1] op_sel_hi:[1,1]\n"
      "s_mov_b32 m0, s60\n"
      "s_mov_b32 s60, s61\n"
      "s_add_u32 s61, s61, 256\n"
      "v_mfma_f32_32x32x16_f16 v[216:231], v[180:183], v[236:239], v[216:231]\n"
      "v_mul_f32 v213, v184, v200\n"
      "v_mul_f32 v214, v185, v201\n"
      "v_fmac_f32 v213, v186, v202\n"
      "v_fmac_f32 v214, v187, v203\n"
      "v_fmac_f32 v213, v188, v204\n"
      "v_mfma_f32_32x32x16_f16 v[216:231], v[4:7], v[246:249], v[216:231]\n"
      "v_fmac_f32 v214, v189, v205\n"
      "v_fmac_f32 v213, v190, v206\n"
      "v_fmac_f32 v214, v191, v207\n"
      "v_fmac_f32 v213, v192, v208\n"
      "v_fmac_f32 v214, v193, v209\n"
      "v_mfma_f32_32x32x16_f16 v[216:231], v[12:15], v[250:253], v[216:231]\n"
      "v_fmac_f32 v213, v194, v210\n"
      "v_fmac_f32 v214, v195, v211\n"
      "v_fmac_f32 v213, v196, v212\n"
      "v_add_f32 v213, v213, v214\n"
      "ds_write_addtid_b32 v213\n"
      "v_mfma_f32_32x32x16_f16 v[184:199], v[0:3], v[246:249], 0\n"
      "v_pk_mul_f16 v246, v237, v232 op_sel:[0,0] op_sel_hi:[1,0]\n"
      "v_perm_b32 v247, v234, v233, s70\n"
      "v_pk_mul_f16 v247, v247, v247\n"
      "v_pk_mul_f16 v248, v237, v232 op_sel:[0,1] op_sel_hi:[1,1]\n"
      "v_pk_mul_f16 v249, v238, v232 op_sel:[0,0] op_sel_hi:[1,0]\n"
      "v_mfma_f32_32x32x16_f16 v[184:199], v[8:11], v[250:253], v[184:199]\n"
      "v_pk_mul_f16 v250, v238, v232 op_sel:[0,1] op_sel_hi:[1,1]\n"
      "v_pk_mul_f16 v251, v233, v235 op_sel:[0,0] op_sel_hi:[1,0]\n"
      "v_mfma_f32_32x32x16_f16 v[184:199], v[16:19], v[246:249], v[184:199]\n"
      "v_pk_mul_f16 v252, v233, v235 op_sel:[0,1] op_sel_hi:[1,1]\n"
      "v_pk_mul_f16 v253, v233, v237 op_sel:[0,0] op_sel_hi:[1,0]\n"
      "v_mfma_f32_32x32x16_f16 v[216:231], v[20:23], v[246:249], v[216:231]\n"
      "v_pk_mul_f16 v246, v233, v237 op_sel:[0,1] op_sel_hi:[1,1]\n"
      "v_perm_b32 v247, v236, v235, s70\n"
      "v_pk_mul_f16 v247, v247, v247\n"
      "v_mfma_f32_32x32x16_f16 v[184:199], v[24:27], v[250:253], v[184:199]\n"
      "v_pk_mul_f16 v248, v234, v235 op_sel:[0,0] op_sel_hi:[1,0]\n"
      "v_pk_mul_f16 v249, v234, v235 op_sel:[0,1] op_sel_hi:[1,1]\n"
      "ds_read_b128 v[200:203], v254 offset:512\n"
      "v_mfma_f32_32x32x16_f16 v[216:231], v[28:31], v[250:253], v[216:231]\n"
      "v_pk_mul_f16 v250, v234, v238 op_sel:[0,0] op_sel_hi:[1,0]\n"
      "v_pk_mul_f16 v251, v234, v238 op_sel:[0,1] op_sel_hi:[1,1]\n"
      "v_mfma_f32_32x32x16_f16 v[184:199], v[32:35], v[246:249], v[184:199]\n"
      "v_pk_mul_f16 v252, v236, v237 op_sel:[0,0] op_sel_hi:[1,0]\n"
      "v_pk_mul_f16 v253, v236, v237 op_sel:[0,1] op_sel_hi:[1,1]\n"
      "v_add_u32 v254, 512, v254\n"
      "v_mfma_f32_32x32x16_f16 v[216:231], v[36:39], v[246:249], v[216:231]\n"
      "v_pk_mul_f16 v246, v236, v238 op_sel:[0,0] op_sel_hi:[1,0]\n"
      "v_perm_b32 v247, v238, v237, s70\n"
      "v_pk_mul_f16 v247, v247, v247\n"
      "v_mfma_f32_32x32x16_f16 v[184:199], v[40:43], v[250:253], v[184:199]\n"
      "v_pk_mul_f16 v248, v236, v238 op_sel:[0,1] op_sel_hi:[1,1]\n"
      "v_pk_mul_f16 v249, v232, v232 op_sel:[0,0] op_sel_hi:[1,0]\n"
      "v_mfma_f32_32x32x16_f16 v[216:231], v[44:47], v[250:253], v[216:231]\n"
      "v_pk_mul_f16 v250, v233, v233 op_sel:[0,0] op_sel_hi:[1,0]\n"
      "v_pk_mul_f16 v251, v234, v234 op_sel:[0,0] op_sel_hi:[1,0]\n"
      "v_mfma_f32_32x32x16_f16 v[184:199], v[48:51], v[246:249], v[184:199]\n"
      "v_pk_mul_f16 v252, v235, v235 op_sel:[0,0] op_sel_hi:[1,0]\n"
      "v_pk_mul_f16 v253, v236, v236 op_sel:[0,0] op_sel_hi:[1,0]\n"
      "v_mfma_f32_32x32x16_f16 v[216:231], v[52:55], v[246:249], v[216:231]\n"
      "v_pk_mul_f16 v246, v237, v237 op_sel:[0,0] op_sel_hi:[1,0]\n"
      "v_pk_mul_f16 v247, v238, v238 op_sel:[0,0] op_sel_hi:[1,0]\n"
      "v_mfma_f32_32x32x16_f16 v[184:199], v[56:59], v[250:253], v[184:199]\n"
      "s_waitcnt lgkmcnt(2)\n"
      "v_pk_mul_f16 v248, v233, v240 op_sel:[0,0] op_sel_hi:[1,0]\n"
      "v_pk_mul_f16 v249, v233, v240 op_sel:[0,1] op_sel_hi:[1,1]\n"
      "v_mfma_f32_32x32x16_f16 v[216:231], v[60:63], v[250:253], v[216:231]\n"
      "v_pk_mul_f16 v250, v233, v241 op_sel:[0,0] op_sel_hi:[1,0]\n"
      "v_pk_mul_f16 v251, v233, v241 op_sel:[0,1] op_sel_hi:[1,1]\n"
      "v_mfma_f32_32x32x16_f16 v[184:199], v[64:67], v[246:249], v[184:199]\n"
      "v_pk_mul_f16 v252, v233, v245 op_sel:[0,0] op_sel_hi:[1,0]\n"
      "v_pk_mul_f16 v253, v233, v245 op_sel:[0,1] op_sel_hi:[1,1]\n"
      "v_mfma_f32_32x32x16_f16 v[216:231], v[68:71], v[246:249], v[216:231]\n"
      "v_pk_mul_f16 v246, v240, v234 op_sel:[0,0] op_sel_hi:[1,0]\n"
      "v_pk_mul_f16 v247, v240, v234 op_sel:[0,1] op_sel_hi:[1,1]\n"
      "v_mfma_f32_32x32x16_f16 v[184:199], v[72:75], v[250:253], v[184:199]\n"
      "v_pk_mul_f16 v248, v240, v238 op_sel:[0,0] op_sel_hi:[1,0]\n"
      "v_pk_mul_f16 v249, v240, v238 op_sel:[0,1] op_sel_hi:[1,1]\n"
      "v_mfma_f32_32x32x16_f16 v[216:231], v[76:79], v[250:253], v[216:231]\n"
      "v_pk_mul_f16 v250, v234, v242 op_sel:[0,0] op_sel_hi:[1,0]\n"
      "v_pk_mul_f16 v251, v234, v242 op_sel:[0,1] op_sel_hi:[1,1]\n"
      "v_mfma_f32_32x32x16_f16 v[184:199], v[80:83], v[246:249], v[184:199]\n"
      "v_pk_mul_f16 v252, v234, v243 op_sel:[0,0] op_sel_hi:[1,0]\n"
      "v_pk_mul_f16 v253, v234, v243 op_sel:[0,1] op_sel_hi:[1,1]\n"
      "v_mfma_f32_32x32x16_f16 v[216:231], v[84:87], v[246:249], v[216:231]\n"
      "v_pk_mul_f16 v246, v235, v241 op_sel:[0,0] op_sel_hi:[1,0]\n"
      "v_pk_mul_f16 v247, v235, v241 op_sel:[0,1] op_sel_hi:[1,1]\n"
      "v_mfma_f32_32x32x16_f16 v[184:199], v[88:91], v[250:253], v[184:199]\n"
      "v_pk_mul_f16 v248, v235, v243 op_sel:[0,0] op_sel_hi:[1,0]\n"
      "v_pk_mul_f16 v249, v235, v243 op_sel:[0,1] op_sel_hi:[1,1]\n"
      "v_mfma_f32_32x32x16_f16 v[216:231], v[92:95], v[250:253], v[216:231]\n"
      "v_pk_mul_f16 v250, v235, v244 op_sel:[0,0] op_sel_hi:[1,0]\n"
      "v_pk_mul_f16 v251, v235, v244 op_sel:[0,1] op_sel_hi:[1,1]\n"
      "v_mfma_f32_32x32x16_f16 v[184:199], v[96:99], v[246:249], v[184:199]\n"
      "v_pk_mul_f16 v252, v241, v236 op_sel:[0,0] op_sel_hi:[1,0]\n"
      "v_pk_mul_f16 v253, v241, v236 op_sel:[0,1] op_sel_hi:[1,1]\n"
      "v_mfma_f32_32x32x16_f16 v[216:231], v[100:103], v[246:249], v[216:231]\n"
      "v_pk_mul_f16 v246, v242, v236 op_sel:[0,0] op_sel_hi:[1,0]\n"
      "v_pk_mul_f16 v247, v242, v236 op_sel:[0,1] op_sel_hi:[1,1]\n"
      "v_mfma_f32_32x32x16_f16 v[184:199], v[104:107], v[250:253], v[184:199]\n"
      "v_pk_mul_f16 v248, v242, v237 op_sel:[0,0] op_sel_hi:[1,0]\n"
      "v_pk_mul_f16 v249, v242, v237 op_sel:[0,1] op_sel_hi:[1,1]\n"
      "v_mfma_f32_32x32x16_f16 v[216:231], v[108:111], v[250:253], v[216:231]\n"
      "v_pk_mul_f16 v250, v243, v237 op_sel:[0,0] op_sel_hi:[1,0]\n"
      "v_pk_mul_f16 v251, v243, v237 op_sel:[0,1] op_sel_hi:[1,1]\n"
      "v_mfma_f32_32x32x16_f16 v[184:199], v[112:115], v[246:249], v[184:199]\n"
      "v_pk_mul_f16 v252, v244, v236 op_sel:[0,0] op_sel_hi:[1,0]\n"
      "v_pk_mul_f16 v253, v244, v236 op_sel:[0,1] op_sel_hi:[1,1]\n"
      "v_mfma_f32_32x32x16_f16 v[216:231], v[116:119], v[246:249], v[216:231]\n"
      "v_pk_mul_f16 v246, v244, v238 op_sel:[0,0] op_sel_hi:[1,0]\n"
      "v_pk_mul_f16 v247, v244, v238 op_sel:[0,1] op_sel_hi:[1,1]\n"
      "v_mfma_f32_32x32x16_f16 v[184:199], v[120:123], v[250:253], v[184:199]\n"
      "v_pk_mul_f16 v248, v237, v245 op_sel:[0,0] op_sel_hi:[1,0]\n"
      "v_pk_mul_f16 v249, v237, v245 op_sel:[0,1] op_sel_hi:[1,1]\n"
      "v_mfma_f32_32x32x16_f16 v[216:231], v[124:127], v[250:253], v[216:231]\n"
      "v_pk_mul_f16 v250, v245, v238 op_sel:[0,0] op_sel_hi:[1,0]\n"
      "v_pk_mul_f16 v251, v245, v238 op_sel:[0,1] op_sel_hi:[1,1]\n"
      "v_mfma_f32_32x32x16_f16 v[184:199], v[128:131], v[246:249], v[184:199]\n"
      "v_pk_mul_f16 v252, v240, v241 op_sel:[0,0] op_sel_hi:[1,0]\n"
      "v_pk_mul_f16 v253, v240, v241 op_sel:[0,1] op_sel_hi:[1,1]\n"
      "s_waitcnt lgkmcnt(0)\n"
      "v_xor_b32_sdwa v232, v200, v255 dst_sel:DWORD dst_unused:UNUSED_PAD src0_sel:WORD_0 src1_sel:DWORD\n"
      "ds_read_b64_tr_b16 v[232:233], v232\n"
      "v_mfma_f32_32x32x16_f16 v[216:231], v[132:135], v[246:249], v[216:231]\n"
      "v_pk_mul_f16 v246, v240, v242 op_sel:[0,0] op_sel_hi:[1,0]\n"
      "v_pk_mul_f16 v247, v240, v242 op_sel:[0,1] op_sel_hi:[1,1]\n"
      "v_xor_b32_sdwa v234, v200, v255 dst_sel:DWORD dst_unused:UNUSED_PAD src0_sel:WORD_1 src1_sel:DWORD\n"
      "ds_read_b64_tr_b16 v[234:235], v234\n"
      "v_mfma_f32_32x32x16_f16 v[184:199], v[136:139], v[250:253], v[184:199]\n"
      "v_pk_mul_f16 v248, v240, v245 op_sel:[0,0] op_sel_hi:[1,0]\n"
      "v_pk_mul_f16 v249, v240, v245 op_sel:[0,1] op_sel_hi:[1,1]\n"
      "v_xor_b32_sdwa v236, v201, v255 dst_sel:DWORD dst_unused:UNUSED_PAD src0_sel:WORD_0 src1_sel:DWORD\n"
      "ds_read_b64_tr_b16 v[236:237], v236\n"
      "v_mfma_f32_32x32x16_f16 v[216:231], v[140:143], v[250:253], v[216:231]\n"
      "v_pk_mul_f16 v250, v241, v242 op_sel:[0,0] op_sel_hi:[1,0]\n"
      "v_pk_mul_f16 v251, v241, v242 op_sel:[0,1] op_sel_hi:[1,1]\n"
      "v_xor_b32_sdwa v238, v201, v255 dst_sel:DWORD dst_unused:UNUSED_PAD src0_sel:WORD_1 src1_sel:DWORD\n"
      "ds_read_b64_tr_b16 v[238:239], v238\n"
      "v_mfma_f32_32x32x16_f16 v[184:199], v[144:147], v[246:249], v[184:199]\n"
      "v_pk_mul_f16 v252, v241, v244 op_sel:[0,0] op_sel_hi:[1,0]\n"
      "v_pk_mul_f16 v253, v241, v244 op_sel:[0,1] op_sel_hi:[1,1]\n"
      "v_mfma_f32_32x32x16_f16 v[216:231], v[148:151], v[246:249], v[216:231]\n"
      "v_pk_mul_f16 v246, v242, v243 op_sel:[0,0] op_sel_hi:[1,0]\n"
      "v_pk_mul_f16 v247, v242, v243 op_sel:[0,1] op_sel_hi:[1,1]\n"
      "v_mfma_f32_32x32x16_f16 v[184:199], v[152:155], v[250:253], v[184:199]\n"
      "v_pk_mul_f16 v248, v243, v244 op_sel:[0,0] op_sel_hi:[1,0]\n"
      "v_pk_mul_f16 v249, v243, v244 op_sel:[0,1] op_sel_hi:[1,1]\n"
      "v_mfma_f32_32x32x16_f16 v[216:231], v[156:159], v[250:253], v[216:231]\n"
      "v_pk_mul_f16 v250, v243, v245 op_sel:[0,0] op_sel_hi:[1,0]\n"
      "v_pk_mul_f16 v251, v243, v245 op_sel:[0,1] op_sel_hi:[1,1]\n"
      "v_mfma_f32_32x32x16_f16 v[184:199], v[160:163], v[246:249], v[184:199]\n"
      "v_pk_mul_f16 v252, v244, v245 op_sel:[0,0] op_sel_hi:[1,0]\n"
      "v_pk_mul_f16 v253, v244, v245 op_sel:[0,1] op_sel_hi:[1,1]\n"
      "v_mfma_f32_32x32x16_f16 v[216:231], v[164:167], v[246:249], v[216:231]\n"
      "s_waitcnt lgkmcnt(0)\n"
      "v_pk_mul_f16 v246, v233, v232 op_sel:[0,0] op_sel_hi:[1,0]\n"
      "v_pk_mul_f16 v247, v233, v232 op_sel:[0,1] op_sel_hi:[1,1]\n"
      "v_xor_b32_sdwa v240, v202, v255 dst_sel:DWORD dst_unused:UNUSED_PAD src0_sel:WORD_0 src1_sel:DWORD\n"
      "ds_read_b64_tr_b16 v[240:241], v240\n"
      "v_mfma_f32_32x32x16_f16 v[184:199], v[168:171], v[250:253], v[184:199]\n"
      "v_pk_mul_f16 v248, v234, v232 op_sel:[0,0] op_sel_hi:[1,0]\n"
      "v_pk_mul_f16 v249, v234, v232 op_sel:[0,1] op_sel_hi:[1,1]\n"
      "v_xor_b32_sdwa v242, v202, v255 dst_sel:DWORD dst_unused:UNUSED_PAD src0_sel:WORD_1 src1_sel:DWORD\n"
      "ds_read_b64_tr_b16 v[242:243], v242\n"
      "s_sub_u32 s62, s62, 1\n"
      "v_mfma_f32_32x32x16_f16 v[216:231], v[172:175], v[250:253], v[216:231]\n"
      "s_cmp_lg_u32 s62, 0\n"
      "v_pk_mul_f16 v250, v235, v232 op_sel:[0,0] op_sel_hi:[1,0]\n"
      "v_pk_mul_f16 v251, v235, v232 op_sel:[0,1] op_sel_hi:[1,1]\n"
      "v_xor_b32_sdwa v244, v203, v255 dst_sel:DWORD dst_unused:UNUSED_PAD src0_sel:WORD_0 src1_sel:DWORD\n"
      "ds_read_b64_tr_b16 v[244:245], v244\n"
      "s_nop 0\n"
      "s_cbranch_scc1 13b\n"
      "s_mov_b32 m0, s60\n"
      "v_mbcnt_lo_u32_b32 v0, -1, 0\n"
      "v_mbcnt_hi_u32_b32 v0, -1, v0\n"
      "v_mov_b32 v1, 61376\n"
      "s_waitcnt lgkmcnt(0)\n"
      "ds_read_b64 v[6:7], v1 offset:48\n"
      "ds_read_b32 v4, v1 offset:40\n"
      "v_lshrrev_b32 v2, 2, v0\n"
      "v_and_b32 v3, 3, v0\n"
      "v_lshrrev_b32 v8, 1, v2\n"
      "v_and_b32 v9, 1, v2\n"
      "v_lshlrev_b32 v8, 8, v8\n"
      "v_lshl_add_u32 v8, v9, 6, v8\n"
      "v_lshl_add_u32 v8, v3, 4, v8\n"
      "v_add_u32 v8, %[pbase], v8\n"
      "v_add_u32 v2, %[smp0], v2\n"
      "v_lshlrev_b32 v10, 2, v2\n"
      "v_and_b32 v11, 0x80, v10\n"
      "v_add_u32 v10, v10, v11\n"
      "v_lshl_add_u32 v10, v3, 9, v10\n"
      "v_add_u32 v10, 104256, v10\n"
      "v_lshlrev_b32 v2, 1, v2\n"
      "v_mul_f32 v229, v184, v216\n"
      "v_mul_f32 v230, v185, v217\n"
      "v_fmac_f32 v229, v186, v218\n"
      "v_fmac_f32 v230, v187, v219\n"
      "v_fmac_f32 v229, v188, v220\n"
      "v_fmac_f32 v230, v189, v221\n"
      "v_fmac_f32 v229, v190, v222\n"
      "v_fmac_f32 v230, v191, v223\n"
      "v_fmac_f32 v229, v192, v224\n"
      "v_fmac_f32 v230, v193, v225\n"
      "v_fmac_f32 v229, v194, v226\n"
      "v_fmac_f32 v230, v195, v227\n"
      "v_fmac_f32 v229, v196, v228\n"
      "v_add_f32 v229, v229, v230\n"
      "ds_write_addtid_b32 v229\n"
      "ds_read_b128 v[12:15], v8\n"
      "ds_read_b128 v[16:19], v8 offset:128\n"
      "s_waitcnt lgkmcnt(3)\n"
      "v_cmp_gt_u32 vcc, 4, v4\n"
      "s_cbranch_vccz 15f\n"
      "14:\n"
      "s_sleep 1\n"
      "ds_read_b32 v4, v1 offset:40\n"
      "s_waitcnt lgkmcnt(0)\n"
      "v_cmp_gt_u32 vcc, 4, v4\n"
      "s_cbranch_vccnz 14b\n"
      "15:\n"
      "ds_read2_b32 v[20:21], v10 offset0:0 offset1:32\n"
      "s_waitcnt lgkmcnt(0)\n"
      "v_add_f32 v12, v12, v16\n"
      "v_add_f32 v13, v13, v17\n"
      "v_add_f32 v14, v14, v18\n"
      "v_add_f32 v15, v15, v19\n"
      "v_add_f32 v20, v20, v21\n"
      "v_add_f32 v12, v12, v13\n"
      "v_add_f32 v14, v14, v15\n"
      "s_mov_b32 s62, 0x11111111\n"
      "v_add_f32 v12, v12, v14\n"
      "s_mov_b32 s63, s62\n"
      "v_ldexp_f32 v12, v12, v6\n"
      "s_nop 0\n"
      "v_add_f32 v12, v12, v20\n"
      "s_nop 1\n"
      "v_add_f32_dpp v12, v12, v12 quad_perm:[1,0,3,2] row_mask:0xf bank_mask:0xf\n"
      "s_nop 1\n"
      "v_add_f32_dpp v12, v12, v12 quad_perm:[2,3,0,1] row_mask:0xf bank_mask:0xf\n"
      "s_nop 0\n"
      "v_add_f32 v12, v12, v7\n"
      "s_nop 0\n"
      "v_cvt_pk_bf16_f32 v12, v12, v12\n"
      "s_mov_b64 exec, s[62:63]\n"
      "global_store_short v2, v12, %[outp]\n"
      "s_mov_b64 exec, -1\n"
      "s_mov_b32 m0, s64\n"
      : "+{v184}"(in.lw[0]), "+{v185}"(in.lw[1]), "+{v186}"(in.lw[2]), "+{v187}"(in.lw[3]), "+{v188}"(in.lw[4]), "+{v189}"(in.go0),
        "+{v190}"(in.go1), "+{v191}"(in.fra), "+{v192}"(in.era), "+{v193}"(in.twa), "+{v254}"(in.pt), "+{v255}"(in.pp)
      : [pbase] "s"(OFFS + c * 2048), [ws] "s"(ws + WS_FRAG), [ex2] "s"(2 * ex), [tb1] "s"(tb1),
        [e0] "s"(tagbits(ep, 0)), [e1] "s"(tagbits(ep, 1)), [e2] "s"(tagbits(ep, 2)), [e3] "s"(tagbits(ep, 3)),
        [smp0] "s"(c * 16), [outp] "s"(out + blk * 64), [lm] "s"(c == 3 ? 0xffffull : ~0ull)
      : "v0", "v1", "v2", "v3", "v4", "v5", "v6", "v7", "v8", "v9", "v10", "v11", "v12", "v13", "v14", "v15", "v16", "v17", "v18", "v19", "v20", "v21", "v22", "v23", "v24", "v25", "v26", "v27", "v28", "v29", "v30", "v31", "v32", "v33", "v34", "v35", "v36", "v37", "v38", "v39", "v40", "v41", "v42", "v43", "v44", "v45", "v46", "v47", "v48", "v49", "v50", "v51", "v52", "v53", "v54", "v55", "v56", "v57", "v58", "v59", "v60", "v61", "v62", "v63", "v64", "v65", "v66", "v67", "v68", "v69", "v70", "v71", "v72", "v73", "v74", "v75", "v76", "v77", "v78", "v79", "v80", "v81", "v82", "v83", "v84", "v85", "v86", "v87", "v88", "v89", "v90", "v91", "v92", "v93", "v94", "v95", "v96", "v97", "v98", "v99", "v100", "v101", "v102", "v103", "v104", "v105", "v106", "v107", "v108", "v109", "v110", "v111", "v112", "v113", "v114", "v115", "v116", "v117", "v118", "v119", "v120", "v121", "v122", "v123", "v124", "v125", "v126", "v127", "v128", "v129", "v130", "v131", "v132", "v133", "v134", "v135", "v136", "v137", "v138", "v139", "v140", "v141", "v142", "v143", "v144", "v145", "v146", "v147", "v148", "v149", "v150", "v151", "v152", "v153", "v154", "v155", "v156", "v157", "v158", "v159", "v160", "v161", "v162", "v163", "v164", "v165", "v166", "v167", "v168", "v169", "v170", "v171", "v172", "v173", "v174", "v175", "v176", "v177", "v178", "v179", "v180", "v181", "v182", "v183", "v194", "v195", "v196", "v197", "v198", "v199", "v200", "v201", "v202", "v203", "v204", "v205", "v206", "v207", "v208", "v209", "v210", "v211", "v212", "v213", "v214", "v215", "v216", "v217", "v218", "v219", "v220", "v221", "v222", "v223", "v224", "v225", "v226", "v227", "v228", "v229", "v230", "v231", "v232", "v233", "v234", "v235", "v236", "v237", "v238", "v239", "v240", "v241", "v242", "v243", "v244", "v245", "v246", "v247", "v248", "v249", "v250", "v251", "v252", "v253", "s60", "s61", "s62", "s63", "s64", "s65", "s66", "s67", "s68", "s69", "s70", "s71", "s72", "s73", "s74", "s75", "s76", "s77", "vcc", "scc", "memory");
  }
}

extern "C" void kernel_launch(void* const* d_in, const int* in_sizes, int n_in, void* d_out, int out_size, void* d_ws, size_t ws_size,
                              hipStream_t stream) {
  xdfm_main<<<256, 512, 0, stream>>>((const int*)d_in[0], (const u16*)d_in[1], (const u16*)d_in[2], (const u16*)d_in[3], (const u16*)d_in[4],
                                     (const u16*)d_in[5], (const u16*)d_in[6], (const u16*)d_in[7], (u16*)d_out, (char*)d_ws);
}
```
